```python
import torch

def _softmax(x, axis=-1):
    return torch.softmax(x, dim=axis)
def _gelu(x):
    return torch.nn.functional.gelu(x, approximate="tanh")
def _silu(x):
    return torch.nn.functional.silu(x)
def _sigmoid(x):
    return torch.sigmoid(x)
def _layernorm(x, axis=-1, eps=1e-5):
    m = x.mean(dim=axis, keepdim=True)
    v = x.var(dim=axis, keepdim=True, unbiased=False)
    return (x - m) / torch.sqrt(v + eps)
def _scatter_add(tgt, idx, src, axis=0):
    return tgt.clone().index_add_(axis, idx, src)
def _segment_sum(src, seg, n):
    out = torch.zeros((n,) + src.shape[1:], dtype=src.dtype)
    return out.index_add_(0, seg, src)
def _pad(x, pw):
    flat = tuple(v for pair in reversed(pw) for v in pair)
    return torch.nn.functional.pad(x, flat)

kernel_name = 'standalone__gqa_softmax__near_ridge'

def setup_inputs():

    _w = (lambda a: (lambda t: t.to(torch.bfloat16) if t.is_floating_point() else t)(torch.as_tensor(a)))
    B, Hq, Hkv, Sq, Sk, D = 32, 32, 16, 384, 512, 128
    return {
        "q": _w((lambda *s: torch.randn(*s, dtype=torch.bfloat16) * 0.8)(B, Hq, Sq, D)),
        "k": _w((lambda *s: torch.randn(*s, dtype=torch.bfloat16) * 0.8)(B, Hkv, Sk, D)),
        "v": _w((lambda *s: torch.randn(*s, dtype=torch.bfloat16) * 0.05)(B, Hkv, Sk, D)),
    }

def reference(q, k, v):
    B, Hq, Sq, D = q.shape
    Hkv = k.shape[1]
    rep = Hq // Hkv
    k = (lambda x, n, axis: torch.repeat_interleave(x, n, dim=axis))(k, rep, axis=1)
    v = (lambda x, n, axis: torch.repeat_interleave(x, n, dim=axis))(v, rep, axis=1)
    scale = D ** -0.5
    scores = torch.einsum("bhqd,bhkd->bhqk", q, k) * scale
    p = _softmax(scores, axis=-1)
    return torch.einsum("bhqk,bhkd->bhqd", p, v)
```

```cpp
#include <hip/hip_runtime.h>
#include <hip/hip_bf16.h>
#include <cstdio>
using bf16 = __hip_bfloat16;
constexpr int   D = 128, NW = 8, QBLK = 32, KVBLK = 64;
constexpr float SCALE = 0.088388347648318440f;
constexpr float THR = 8.f;
constexpr int SDEPTH = 2;
constexpr int LDQ = D, LDK = D, LDO = D;
constexpr size_t SHM_V = KVBLK * D * 2, SHM_K = KVBLK * D * 2, SHM_ATTN = 2 * SHM_V + 2 * SHM_K + NW * 64 * 4;
using bf16x8 = __attribute__((ext_vector_type(8))) short;
using s16x4  = __attribute__((ext_vector_type(4))) short;
using f32x16 = __attribute__((ext_vector_type(16))) float;
using f32x8  = __attribute__((ext_vector_type(8))) float;
using u32x4  = __attribute__((ext_vector_type(4))) unsigned;
#define KSWZ(row, colB) ((row) * 256 + ((colB) ^ (((row) & 7) << 4)))
#define SBAR() __builtin_amdgcn_sched_barrier(0)
__device__ __forceinline__ int crow(int r, int hi) { return (r & 3) + 8 * (r >> 2) + 4 * hi; }
__device__ __forceinline__ unsigned cvtpk(float lo, float hi) {
  unsigned r; asm volatile("v_cvt_pk_bf16_f32 %0, %1, %2" : "=v"(r) : "v"(lo), "v"(hi)); return r;
}
__device__ __forceinline__ bf16x8 ld8(const bf16* p) { return *reinterpret_cast<const bf16x8*>(p); }
__device__ __forceinline__ void partialSM(f32x16& p0, f32x16& p1, float& m_reg, float& mn, float& alpha) {
  constexpr float C = SCALE * 1.4426950408889634f;
  float pmax = p0[0]; for (int r = 1; r < 16; ++r) pmax = fmaxf(pmax, p0[r]); for (int r = 0; r < 16; ++r) pmax = fmaxf(pmax, p1[r]);
  { auto rr = __builtin_amdgcn_permlane32_swap(__float_as_uint(pmax), __float_as_uint(pmax), false, false);
    pmax = fmaxf(__uint_as_float(rr[0]), __uint_as_float(rr[1])); }
  if (__builtin_expect(__all(pmax - m_reg <= THR / SCALE), 1)) { mn = m_reg; alpha = 1.f; }
  else { mn = fmaxf(m_reg, pmax); alpha = __builtin_amdgcn_exp2f((m_reg - mn) * C); m_reg = mn; }
  float mnC = -mn * C;
  for (int r = 0; r < 16; ++r) p0[r] = fmaf(p0[r], C, mnC); for (int r = 0; r < 16; ++r) p1[r] = fmaf(p1[r], C, mnC);
  for (int r = 0; r < 16; ++r) p0[r] = __builtin_amdgcn_exp2f(p0[r]);
}
__device__ __forceinline__ void finishSM(f32x16& p0, f32x16& p1, float alpha, float& l_reg, bf16x8& pa0, bf16x8& pa1, bf16x8& pa2, bf16x8& pa3) {
  for (int r = 0; r < 16; ++r) p1[r] = __builtin_amdgcn_exp2f(p1[r]);
  float ps = 0; for (int r = 0; r < 16; ++r) ps += p0[r]; for (int r = 0; r < 16; ++r) ps += p1[r];
  { auto rr = __builtin_amdgcn_permlane32_swap(__float_as_uint(ps), __float_as_uint(ps), false, false);
    ps = __uint_as_float(rr[0]) + __uint_as_float(rr[1]); }
  l_reg = l_reg * alpha + ps;
#define PK4(P, BASE, OUT) do { unsigned a0 = cvtpk(P[BASE + 0], P[BASE + 1]), a1 = cvtpk(P[BASE + 2], P[BASE + 3]);   \
    unsigned b0 = cvtpk(P[BASE + 4], P[BASE + 5]), b1 = cvtpk(P[BASE + 6], P[BASE + 7]);                              \
    auto r0 = __builtin_amdgcn_permlane32_swap(a0, b0, false, false); auto r1 = __builtin_amdgcn_permlane32_swap(a1, b1, false, false); \
    u32x4 w = {r0[0], r1[0], r0[1], r1[1]}; OUT = *reinterpret_cast<bf16x8*>(&w); } while (0)
  PK4(p0, 0, pa0); PK4(p0, 8, pa1); PK4(p1, 0, pa2); PK4(p1, 8, pa3);
#undef PK4
}
__device__ __forceinline__ void qkt(f32x16& p0, f32x16& p1, const bf16* Ks, const bf16x8* qr, int r32, int hi) {
  p0 = f32x16{}; p1 = f32x16{};
  for (int d0 = 0; d0 < 8; ++d0) { int cb = (d0 * 16 + hi * 8) * 2;
    bf16x8 b0 = *reinterpret_cast<const bf16x8*>((const char*)Ks + KSWZ(r32, cb));
    bf16x8 b1 = *reinterpret_cast<const bf16x8*>((const char*)Ks + KSWZ(32 + r32, cb));
    p0 = __builtin_amdgcn_mfma_f32_32x32x16_bf16(b0, qr[d0], p0, 0, 0, 0);
    p1 = __builtin_amdgcn_mfma_f32_32x32x16_bf16(b1, qr[d0], p1, 0, 0, 0); }
}
__device__ __forceinline__ int v_st(int k, int c) { const int kk = (k & ~0xC) | ((k & 4) << 1) | ((k & 8) >> 1); return ((kk >> 3) * 4 + (c >> 5)) * 512 + ((kk & 7) * 32 + (c & 31)) * 2; }
__device__ __forceinline__ int v_rd_base(int lane) { return ((lane & 3) << 3) | (((lane >> 2) & 3) << 6) | (((lane >> 4) & 1) << 5) | (((lane >> 5) & 1) << 8); }
constexpr int v_rd_off(int d0, int ks, int half) { return d0 * 512 + ks * 4096 + half * 2048; }
template <int OFF> __device__ __forceinline__ s16x4 tr_read(int vb) {
  s16x4 r; asm volatile("ds_read_b64_tr_b16 %0, %1 offset:%2" : "=&v"(r) : "v"(vb), "i"(OFF) : "memory"); return r;
}
template <int D0> __device__ __forceinline__ void pv_one(f32x16& od, int vb, bf16x8 pa0, bf16x8 pa1, bf16x8 pa2, bf16x8 pa3) {
  const s16x4 l0 = tr_read<v_rd_off(D0, 0, 0)>(vb), h0 = tr_read<v_rd_off(D0, 0, 1)>(vb), l1 = tr_read<v_rd_off(D0, 1, 0)>(vb), h1 = tr_read<v_rd_off(D0, 1, 1)>(vb);
  const s16x4 l2 = tr_read<v_rd_off(D0, 2, 0)>(vb), h2 = tr_read<v_rd_off(D0, 2, 1)>(vb), l3 = tr_read<v_rd_off(D0, 3, 0)>(vb), h3 = tr_read<v_rd_off(D0, 3, 1)>(vb);
  asm volatile("s_waitcnt lgkmcnt(0)" ::: "memory"); SBAR();
#define PK(L, H) (bf16x8){L[0], L[1], L[2], L[3], H[0], H[1], H[2], H[3]}
  od = __builtin_amdgcn_mfma_f32_32x32x16_bf16(pa0, PK(l0, h0), od, 0, 0, 0);
  od = __builtin_amdgcn_mfma_f32_32x32x16_bf16(pa1, PK(l1, h1), od, 0, 0, 0);
  od = __builtin_amdgcn_mfma_f32_32x32x16_bf16(pa2, PK(l2, h2), od, 0, 0, 0);
  od = __builtin_amdgcn_mfma_f32_32x32x16_bf16(pa3, PK(l3, h3), od, 0, 0, 0);
#undef PK
}
__device__ __forceinline__ void pv_d0(f32x16* o, int vb, bf16x8 pa0, bf16x8 pa1, bf16x8 pa2, bf16x8 pa3) {
  pv_one<0>(o[0], vb, pa0, pa1, pa2, pa3); pv_one<1>(o[1], vb, pa0, pa1, pa2, pa3); pv_one<2>(o[2], vb, pa0, pa1, pa2, pa3); pv_one<3>(o[3], vb, pa0, pa1, pa2, pa3);
}
__device__ __forceinline__ void attn_dense_body(const bf16* __restrict__ Qb, const bf16* __restrict__ Kh, const bf16* __restrict__ Vh,
                                                bf16* __restrict__ Ob, int seq, char* lds) {
  const int tid = threadIdx.x, wid = tid >> 6, lane = tid & 63, r32 = lane & 31, hi = lane >> 5;
  bf16* V_lds = (bf16*)lds; bf16* K_lds = (bf16*)(lds + 2 * SHM_V);
  float* ws = (float*)(lds + 2 * SHM_V + 2 * SHM_K) + wid * 64; float* li_l = ws; float* al_l = ws + 32;
  float m_reg = -1e30f, l_reg = 0; f32x16 o[4] = {}; bf16x8 qr[8];
  const bf16* Qw = Qb + (long)(wid * QBLK + r32) * LDQ + hi * 8;
#pragma unroll
  for (int d0 = 0; d0 < 8; ++d0) qr[d0] = ld8(Qw + d0 * 16);
  const int sr = tid >> 4, sc = (tid & 15) * 8, vst0 = v_st(sr, sc), vst1 = v_st(32 + sr, sc);
  const int vb0 = (int)(uintptr_t)V_lds + v_rd_base(lane);
  struct { bf16x8 vs0, vs1, ks0, ks1; } sr_[SDEPTH];
#define SLOAD(i, k0) do { sr_[i].vs0 = ld8(&Vh[(long)((k0) + sr) * LDK + sc]); sr_[i].vs1 = ld8(&Vh[(long)((k0) + 32 + sr) * LDK + sc]); \
    sr_[i].ks0 = ld8(&Kh[(long)((k0) + sr) * LDK + sc]); sr_[i].ks1 = ld8(&Kh[(long)((k0) + 32 + sr) * LDK + sc]); } while (0)
#define SWRITE(b, i) do { *(bf16x8*)((char*)V_lds + (b) * SHM_V + vst0) = (sr_[i].vs0);          \
    *(bf16x8*)((char*)V_lds + (b) * SHM_V + vst1) = (sr_[i].vs1); int kc = sc * 2;               \
    *(bf16x8*)((char*)K_lds + (b) * SHM_K + KSWZ(sr, kc)) = (sr_[i].ks0);                       \
    *(bf16x8*)((char*)K_lds + (b) * SHM_K + KSWZ(32 + sr, kc)) = (sr_[i].ks1); } while (0)
#define SWAIT() do { asm volatile("s_waitcnt vmcnt(4)" ::: "memory"); } while (0)
#define RESC(a) do { if (__any((a) < 1.f)) { if (hi == 0) al_l[r32] = (a); asm volatile("s_waitcnt lgkmcnt(0)" ::: "memory"); \
    for (int d = 0; d < 4; ++d) for (int r = 0; r < 16; ++r) o[d][r] *= al_l[crow(r, hi)]; } } while (0)
  f32x16 pA0, pA1, pB0, pB1; float mnA, mnB, alA, alB; bf16x8 pa0, pa1, pa2, pa3; const int NT = seq / KVBLK;
  constexpr int SE = 0, SO = SDEPTH - 1;
  SLOAD(SE, 0); asm volatile("s_waitcnt vmcnt(0)" ::: "memory"); SWRITE(0, SE); __syncthreads();
  qkt(pA0, pA1, K_lds, qr, r32, hi); partialSM(pA0, pA1, m_reg, mnA, alA);
  SLOAD(SO, KVBLK); if (2 < NT) SLOAD(SE, 2 * KVBLK);
  SWAIT(); SWRITE(1, SO); __syncthreads();
  for (int j = 1; j + 1 < NT; j += 2) {
    SBAR(); qkt(pB0, pB1, (bf16*)((char*)K_lds + SHM_K), qr, r32, hi);
    finishSM(pA0, pA1, alA, l_reg, pa0, pa1, pa2, pa3); SBAR();
    SLOAD(SO, (j + SDEPTH) * KVBLK); SBAR();
    pv_d0(o, vb0, pa0, pa1, pa2, pa3); partialSM(pB0, pB1, m_reg, mnB, alB);
    __syncthreads(); SWAIT(); SWRITE(0, SE);
    RESC(alB); __syncthreads();
    SBAR(); qkt(pA0, pA1, K_lds, qr, r32, hi);
    finishSM(pB0, pB1, alB, l_reg, pa0, pa1, pa2, pa3); SBAR();
    if (j + 3 < NT) SLOAD(SE, (j + 1 + SDEPTH) * KVBLK); SBAR();
    pv_d0(o, vb0 + (int)SHM_V, pa0, pa1, pa2, pa3); partialSM(pA0, pA1, m_reg, mnA, alA);
    __syncthreads(); if (j + 3 < NT) { SWAIT(); } else { asm volatile("s_waitcnt vmcnt(0)" ::: "memory"); } SWRITE(1, SO);
    RESC(alA); __syncthreads();
  }
  SBAR(); qkt(pB0, pB1, (bf16*)((char*)K_lds + SHM_K), qr, r32, hi);
  finishSM(pA0, pA1, alA, l_reg, pa0, pa1, pa2, pa3); SBAR();
  pv_d0(o, vb0, pa0, pa1, pa2, pa3); partialSM(pB0, pB1, m_reg, mnB, alB);
  __syncthreads(); RESC(alB);
  finishSM(pB0, pB1, alB, l_reg, pa0, pa1, pa2, pa3); SBAR();
  pv_d0(o, vb0 + (int)SHM_V, pa0, pa1, pa2, pa3);
  if (hi == 0) li_l[r32] = l_reg; asm volatile("s_waitcnt lgkmcnt(0)" ::: "memory");
  float rli[16];
#pragma unroll
  for (int r = 0; r < 16; ++r) rli[r] = __builtin_amdgcn_rcpf(li_l[crow(r, hi)]);
  bf16* Ow = Ob + (long)(wid * QBLK) * LDO;
#pragma unroll
  for (int r = 0; r < 16; ++r) { int orow = crow(r, hi);
    for (int d0 = 0; d0 < 4; ++d0) Ow[(long)orow * LDO + d0 * 32 + r32] = __float2bfloat16(o[d0][r] * rli[r]); }
}
__global__ __launch_bounds__(64 * NW, 1)
void attn_fwd_dense(const bf16* __restrict__ Q, const bf16* __restrict__ K, const bf16* __restrict__ V, bf16* __restrict__ O) {
  extern __shared__ __attribute__((aligned(16))) char shm[];
  const int blk = blockIdx.x, x = blk & 7, i = blk >> 3;
  const int pair = x * 64 + i / 3, qb = i % 3;
  const long q0 = ((long)pair * 768 + qb * 256) * D, k0 = (long)pair * 512 * D;
  attn_dense_body(Q + q0, K + k0, V + k0, O + q0, 512, shm);
}
extern "C" void kernel_launch(void* const* d_in, const int* in_sizes, int n_in, void* d_out, int out_size, void* d_ws, size_t ws_size, hipStream_t stream) {
  hipFuncSetAttribute((const void*)attn_fwd_dense, hipFuncAttributeMaxDynamicSharedMemorySize, (int)SHM_ATTN);
  hipLaunchKernelGGL(attn_fwd_dense, dim3(1536), dim3(64 * NW), SHM_ATTN, stream,
                     (const bf16*)d_in[0], (const bf16*)d_in[1], (const bf16*)d_in[2], (bf16*)d_out);
}
```

```cpp
#include <hip/hip_runtime.h>
#include <hip/hip_bf16.h>
#include <cstdio>
using bf16 = __hip_bfloat16;
constexpr int   D = 128, NW = 8, QBLK = 32, KVBLK = 64;
constexpr float SCALE = 0.088388347648318440f;
constexpr float THR = 8.f;
constexpr int SDEPTH = 2;
constexpr int LDQ = D, LDK = D, LDO = D;
constexpr size_t SHM_V = KVBLK * D * 2, SHM_K = KVBLK * D * 2, SHM_ATTN = 2 * SHM_V + 2 * SHM_K + NW * 64 * 4;
using bf16x8 = __attribute__((ext_vector_type(8))) short;
using s16x4  = __attribute__((ext_vector_type(4))) short;
using f32x16 = __attribute__((ext_vector_type(16))) float;
using f32x8  = __attribute__((ext_vector_type(8))) float;
using u32x4  = __attribute__((ext_vector_type(4))) unsigned;
#define KSWZ(row, colB) ((row) * 256 + ((colB) ^ (((row) & 7) << 4)))
#define SBAR() __builtin_amdgcn_sched_barrier(0)
__device__ __forceinline__ int crow(int r, int hi) { return (r & 3) + 8 * (r >> 2) + 4 * hi; }
__device__ __forceinline__ unsigned cvtpk(float lo, float hi) {
  unsigned r; asm volatile("v_cvt_pk_bf16_f32 %0, %1, %2" : "=v"(r) : "v"(lo), "v"(hi)); return r;
}
__device__ __forceinline__ bf16x8 ld8(const bf16* p) { return *reinterpret_cast<const bf16x8*>(p); }
__device__ __forceinline__ void partialSM(f32x16& p0, f32x16& p1, float& m_reg, float& mn, float& alpha) {
  constexpr float C = SCALE * 1.4426950408889634f;
  float pmax = p0[0]; for (int r = 1; r < 16; ++r) pmax = fmaxf(pmax, p0[r]); for (int r = 0; r < 16; ++r) pmax = fmaxf(pmax, p1[r]);
  { auto rr = __builtin_amdgcn_permlane32_swap(__float_as_uint(pmax), __float_as_uint(pmax), false, false);
    pmax = fmaxf(__uint_as_float(rr[0]), __uint_as_float(rr[1])); }
  if (__builtin_expect(__all(pmax - m_reg <= THR / SCALE), 1)) { mn = m_reg; alpha = 1.f; }
  else { mn = fmaxf(m_reg, pmax); alpha = __builtin_amdgcn_exp2f((m_reg - mn) * C); m_reg = mn; }
  float mnC = -mn * C;
  for (int r = 0; r < 16; ++r) p0[r] = fmaf(p0[r], C, mnC); for (int r = 0; r < 16; ++r) p1[r] = fmaf(p1[r], C, mnC);
  for (int r = 0; r < 16; ++r) p0[r] = __builtin_amdgcn_exp2f(p0[r]);
}
__device__ __forceinline__ void finishSM(f32x16& p0, f32x16& p1, float alpha, float& l_reg, bf16x8& pa0, bf16x8& pa1, bf16x8& pa2, bf16x8& pa3) {
  for (int r = 0; r < 16; ++r) p1[r] = __builtin_amdgcn_exp2f(p1[r]);
  float ps = 0; for (int r = 0; r < 16; ++r) ps += p0[r]; for (int r = 0; r < 16; ++r) ps += p1[r];
  { auto rr = __builtin_amdgcn_permlane32_swap(__float_as_uint(ps), __float_as_uint(ps), false, false);
    ps = __uint_as_float(rr[0]) + __uint_as_float(rr[1]); }
  l_reg = l_reg * alpha + ps;
#define PK4(P, BASE, OUT) do { unsigned a0 = cvtpk(P[BASE + 0], P[BASE + 1]), a1 = cvtpk(P[BASE + 2], P[BASE + 3]);   \
    unsigned b0 = cvtpk(P[BASE + 4], P[BASE + 5]), b1 = cvtpk(P[BASE + 6], P[BASE + 7]);                              \
    auto r0 = __builtin_amdgcn_permlane32_swap(a0, b0, false, false); auto r1 = __builtin_amdgcn_permlane32_swap(a1, b1, false, false); \
    u32x4 w = {r0[0], r1[0], r0[1], r1[1]}; OUT = *reinterpret_cast<bf16x8*>(&w); } while (0)
  PK4(p0, 0, pa0); PK4(p0, 8, pa1); PK4(p1, 0, pa2); PK4(p1, 8, pa3);
#undef PK4
}
__device__ __forceinline__ void qkt(f32x16& p0, f32x16& p1, const bf16* Ks, const bf16x8* qr, int r32, int hi) {
  p0 = f32x16{}; p1 = f32x16{};
  for (int d0 = 0; d0 < 8; ++d0) { int cb = (d0 * 16 + hi * 8) * 2;
    bf16x8 b0 = *reinterpret_cast<const bf16x8*>((const char*)Ks + KSWZ(r32, cb));
    bf16x8 b1 = *reinterpret_cast<const bf16x8*>((const char*)Ks + KSWZ(32 + r32, cb));
    p0 = __builtin_amdgcn_mfma_f32_32x32x16_bf16(b0, qr[d0], p0, 0, 0, 0);
    p1 = __builtin_amdgcn_mfma_f32_32x32x16_bf16(b1, qr[d0], p1, 0, 0, 0); }
}
__device__ __forceinline__ int v_st(int k, int c) { const int kk = (k & ~0xC) | ((k & 4) << 1) | ((k & 8) >> 1); return ((kk >> 3) * 4 + (c >> 5)) * 512 + ((kk & 7) * 32 + (c & 31)) * 2; }
__device__ __forceinline__ int v_rd_base(int lane) { return ((lane & 3) << 3) | (((lane >> 2) & 3) << 6) | (((lane >> 4) & 1) << 5) | (((lane >> 5) & 1) << 8); }
constexpr int v_rd_off(int d0, int ks, int half) { return d0 * 512 + ks * 4096 + half * 2048; }
template <int OFF> __device__ __forceinline__ s16x4 tr_read(int vb) {
  s16x4 r; asm volatile("ds_read_b64_tr_b16 %0, %1 offset:%2" : "=&v"(r) : "v"(vb), "i"(OFF) : "memory"); return r;
}
template <int D0> __device__ __forceinline__ void pv_one(f32x16& od, int vb, bf16x8 pa0, bf16x8 pa1, bf16x8 pa2, bf16x8 pa3) {
  const s16x4 l0 = tr_read<v_rd_off(D0, 0, 0)>(vb), h0 = tr_read<v_rd_off(D0, 0, 1)>(vb), l1 = tr_read<v_rd_off(D0, 1, 0)>(vb), h1 = tr_read<v_rd_off(D0, 1, 1)>(vb);
  const s16x4 l2 = tr_read<v_rd_off(D0, 2, 0)>(vb), h2 = tr_read<v_rd_off(D0, 2, 1)>(vb), l3 = tr_read<v_rd_off(D0, 3, 0)>(vb), h3 = tr_read<v_rd_off(D0, 3, 1)>(vb);
  asm volatile("s_waitcnt lgkmcnt(0)" ::: "memory"); SBAR();
#define PK(L, H) (bf16x8){L[0], L[1], L[2], L[3], H[0], H[1], H[2], H[3]}
  od = __builtin_amdgcn_mfma_f32_32x32x16_bf16(pa0, PK(l0, h0), od, 0, 0, 0);
  od = __builtin_amdgcn_mfma_f32_32x32x16_bf16(pa1, PK(l1, h1), od, 0, 0, 0);
  od = __builtin_amdgcn_mfma_f32_32x32x16_bf16(pa2, PK(l2, h2), od, 0, 0, 0);
  od = __builtin_amdgcn_mfma_f32_32x32x16_bf16(pa3, PK(l3, h3), od, 0, 0, 0);
#undef PK
}
__device__ __forceinline__ void pv_d0(f32x16* o, int vb, bf16x8 pa0, bf16x8 pa1, bf16x8 pa2, bf16x8 pa3) {
  pv_one<0>(o[0], vb, pa0, pa1, pa2, pa3); pv_one<1>(o[1], vb, pa0, pa1, pa2, pa3); pv_one<2>(o[2], vb, pa0, pa1, pa2, pa3); pv_one<3>(o[3], vb, pa0, pa1, pa2, pa3);
}
__device__ __forceinline__ void attn_dense_body(const bf16* __restrict__ Qb, const bf16* __restrict__ Kh, const bf16* __restrict__ Vh,
                                                bf16* __restrict__ Ob, int seq, char* lds) {
  const int tid = threadIdx.x, wid = tid >> 6, lane = tid & 63, r32 = lane & 31, hi = lane >> 5;
  bf16* V_lds = (bf16*)lds; bf16* K_lds = (bf16*)(lds + 2 * SHM_V);
  float* ws = (float*)(lds + 2 * SHM_V + 2 * SHM_K) + wid * 64; float* li_l = ws; float* al_l = ws + 32;
  float m_reg = -1e30f, l_reg = 0; f32x16 o[4] = {}; bf16x8 qr[8];
  const bf16* Qw = Qb + (long)(wid * QBLK + r32) * LDQ + hi * 8;
#pragma unroll
  for (int d0 = 0; d0 < 8; ++d0) qr[d0] = ld8(Qw + d0 * 16);
  const int sr = tid >> 4, sc = (tid & 15) * 8, vst0 = v_st(sr, sc), vst1 = v_st(32 + sr, sc);
  const int vb0 = (int)(uintptr_t)V_lds + v_rd_base(lane);
  struct { bf16x8 vs0, vs1, ks0, ks1; } sr_[SDEPTH];
#define SLOAD(i, k0) do { sr_[i].vs0 = ld8(&Vh[(long)((k0) + sr) * LDK + sc]); sr_[i].vs1 = ld8(&Vh[(long)((k0) + 32 + sr) * LDK + sc]); \
    sr_[i].ks0 = ld8(&Kh[(long)((k0) + sr) * LDK + sc]); sr_[i].ks1 = ld8(&Kh[(long)((k0) + 32 + sr) * LDK + sc]); } while (0)
#define SWRITE(b, i) do { *(bf16x8*)((char*)V_lds + (b) * SHM_V + vst0) = (sr_[i].vs0);          \
    *(bf16x8*)((char*)V_lds + (b) * SHM_V + vst1) = (sr_[i].vs1); int kc = sc * 2;               \
    *(bf16x8*)((char*)K_lds + (b) * SHM_K + KSWZ(sr, kc)) = (sr_[i].ks0);                       \
    *(bf16x8*)((char*)K_lds + (b) * SHM_K + KSWZ(32 + sr, kc)) = (sr_[i].ks1); } while (0)
#define SWAIT() do { asm volatile("s_waitcnt vmcnt(4)" ::: "memory"); } while (0)
#define RESC(a) do { if (__any((a) < 1.f)) { if (hi == 0) al_l[r32] = (a); asm volatile("s_waitcnt lgkmcnt(0)" ::: "memory"); \
    for (int d = 0; d < 4; ++d) for (int r = 0; r < 16; ++r) o[d][r] *= al_l[crow(r, hi)]; } } while (0)
  f32x16 pA0, pA1, pB0, pB1; float mnA, mnB, alA, alB; bf16x8 pa0, pa1, pa2, pa3; const int NT = seq / KVBLK;
  constexpr int SE = 0, SO = SDEPTH - 1;
  SLOAD(SE, 0); asm volatile("s_waitcnt vmcnt(0)" ::: "memory"); SWRITE(0, SE); __syncthreads();
  qkt(pA0, pA1, K_lds, qr, r32, hi); partialSM(pA0, pA1, m_reg, mnA, alA);
  SLOAD(SO, KVBLK); if (2 < NT) SLOAD(SE, 2 * KVBLK);
  SWAIT(); SWRITE(1, SO); __syncthreads();
  for (int j = 1; j + 1 < NT; j += 2) {
    SBAR(); qkt(pB0, pB1, (bf16*)((char*)K_lds + SHM_K), qr, r32, hi);
    finishSM(pA0, pA1, alA, l_reg, pa0, pa1, pa2, pa3); SBAR();
    SLOAD(SO, (j + SDEPTH) * KVBLK); SBAR();
    pv_d0(o, vb0, pa0, pa1, pa2, pa3); partialSM(pB0, pB1, m_reg, mnB, alB);
    __syncthreads(); SWAIT(); SWRITE(0, SE);
    RESC(alB); __syncthreads();
    SBAR(); qkt(pA0, pA1, K_lds, qr, r32, hi);
    finishSM(pB0, pB1, alB, l_reg, pa0, pa1, pa2, pa3); SBAR();
    if (j + 3 < NT) SLOAD(SE, (j + 1 + SDEPTH) * KVBLK); SBAR();
    pv_d0(o, vb0 + (int)SHM_V, pa0, pa1, pa2, pa3); partialSM(pA0, pA1, m_reg, mnA, alA);
    __syncthreads(); if (j + 3 < NT) { SWAIT(); } else { asm volatile("s_waitcnt vmcnt(0)" ::: "memory"); } SWRITE(1, SO);
    RESC(alA); __syncthreads();
  }
  SBAR(); qkt(pB0, pB1, (bf16*)((char*)K_lds + SHM_K), qr, r32, hi);
  finishSM(pA0, pA1, alA, l_reg, pa0, pa1, pa2, pa3); SBAR();
  pv_d0(o, vb0, pa0, pa1, pa2, pa3); partialSM(pB0, pB1, m_reg, mnB, alB);
  __syncthreads(); RESC(alB);
  finishSM(pB0, pB1, alB, l_reg, pa0, pa1, pa2, pa3); SBAR();
  pv_d0(o, vb0 + (int)SHM_V, pa0, pa1, pa2, pa3);
  if (hi == 0) li_l[r32] = l_reg; asm volatile("s_waitcnt lgkmcnt(0)" ::: "memory");
  float rli[16];
#pragma unroll
  for (int r = 0; r < 16; ++r) rli[r] = __builtin_amdgcn_rcpf(li_l[crow(r, hi)]);
  bf16* Ow = Ob + (long)(wid * QBLK) * LDO;
#pragma unroll
  for (int r = 0; r < 16; ++r) { int orow = crow(r, hi);
    for (int d0 = 0; d0 < 4; ++d0) Ow[(long)orow * LDO + d0 * 32 + r32] = __float2bfloat16(o[d0][r] * rli[r]); }
}
__global__ __launch_bounds__(64 * NW, 1)
void attn_fwd_dense(const bf16* __restrict__ Q, const bf16* __restrict__ K, const bf16* __restrict__ V, bf16* __restrict__ O) {
  extern __shared__ __attribute__((aligned(16))) char shm[];
  const int blk = blockIdx.x, x = blk & 7, i = blk >> 3;
  const int pair = x * 64 + i / 3, qb = i % 3;
  const long q0 = ((long)pair * 768 + qb * 256) * D, k0 = (long)pair * 512 * D;
  attn_dense_body(Q + q0, K + k0, V + k0, O + q0, 512, shm);
}
template <int QAUX, int KVAUX, int OAUX, int DEPTH, int SHARE>
__global__ __launch_bounds__(256, 1) void memk(const char* Q, const char* K, const char* V, char* O, unsigned long long* stamps) {
  extern __shared__ __attribute__((aligned(16))) char lds[];
  unsigned tid = threadIdx.x, lane = tid & 63, wid = __builtin_amdgcn_readfirstlane(tid >> 6);
  unsigned long long r0 = __builtin_amdgcn_s_memrealtime();
  unsigned x = blockIdx.x & 7, i = blockIdx.x >> 3;
  auto rq = __builtin_amdgcn_make_buffer_rsrc((void*)Q, 0, 100663296, 0x20000);
  auto rk = __builtin_amdgcn_make_buffer_rsrc((void*)K, 0, 67108864, 0x20000);
  auto rv = __builtin_amdgcn_make_buffer_rsrc((void*)V, 0, 67108864, 0x20000);
  auto ro = __builtin_amdgcn_make_buffer_rsrc((void*)O, 0, 100663296, 0x20000);
  unsigned voff = lane * 16;
  unsigned slot = 0;
  u32x4 val = {tid, tid, tid, tid};
  for (int r = 0; r < 4; r++) {
    unsigned head, pair;
    if (SHARE) { head = x * 128 + r * 32 + i; pair = head >> 1; }
    else { pair = x * 64 + (r >> 1) * 32 + i; head = pair * 2 + (r & 1); }
    unsigned qb = head * 98304u + wid * 24576u, kb = pair * 131072u + wid * 1024u;
    for (int t = 0; t < 8; t++) {
      for (int p = 0; p < 4; p++) {
        unsigned m0 = __builtin_amdgcn_readfirstlane((unsigned)(size_t)lds + ((slot & 31) * 4096) + wid * 1024); slot++;
        unsigned so = __builtin_amdgcn_readfirstlane(kb + t * 16384 + p * 4096);
        if (KVAUX == 0) asm volatile("s_mov_b32 m0, %0\n s_nop 0\n buffer_load_dwordx4 %1, %2, %3 offen lds" :: "s"(m0), "v"(voff), "s"(rk), "s"(so) : "memory");
        else asm volatile("s_mov_b32 m0, %0\n s_nop 0\n buffer_load_dwordx4 %1, %2, %3 offen nt lds" :: "s"(m0), "v"(voff), "s"(rk), "s"(so) : "memory");
        m0 = __builtin_amdgcn_readfirstlane((unsigned)(size_t)lds + ((slot & 31) * 4096) + wid * 1024); slot++;
        if (KVAUX == 0) asm volatile("s_mov_b32 m0, %0\n s_nop 0\n buffer_load_dwordx4 %1, %2, %3 offen lds" :: "s"(m0), "v"(voff), "s"(rv), "s"(so) : "memory");
        else asm volatile("s_mov_b32 m0, %0\n s_nop 0\n buffer_load_dwordx4 %1, %2, %3 offen nt lds" :: "s"(m0), "v"(voff), "s"(rv), "s"(so) : "memory");
        if (p < 3) {
          m0 = __builtin_amdgcn_readfirstlane((unsigned)(size_t)lds + ((slot & 31) * 4096) + wid * 1024); slot++;
          so = __builtin_amdgcn_readfirstlane(qb + (t * 3 + p) * 1024);
          if (QAUX == 0) asm volatile("s_mov_b32 m0, %0\n s_nop 0\n buffer_load_dwordx4 %1, %2, %3 offen lds" :: "s"(m0), "v"(voff), "s"(rq), "s"(so) : "memory");
          else asm volatile("s_mov_b32 m0, %0\n s_nop 0\n buffer_load_dwordx4 %1, %2, %3 offen nt lds" :: "s"(m0), "v"(voff), "s"(rq), "s"(so) : "memory");
          if (OAUX == 0) asm volatile("buffer_store_dwordx4 %0, %1, %2, %3 offen" :: "v"(val), "v"(voff), "s"(ro), "s"(so) : "memory");
          else if (OAUX == 1) asm volatile("buffer_store_dwordx4 %0, %1, %2, %3 offen nt" :: "v"(val), "v"(voff), "s"(ro), "s"(so) : "memory");
          else if (OAUX == 2) asm volatile("buffer_store_dwordx4 %0, %1, %2, %3 offen sc1" :: "v"(val), "v"(voff), "s"(ro), "s"(so) : "memory");
          else if (OAUX == 3) asm volatile("buffer_store_dwordx4 %0, %1, %2, %3 offen sc0 sc1" :: "v"(val), "v"(voff), "s"(ro), "s"(so) : "memory");
          else if (OAUX == 4) asm volatile("buffer_store_dwordx4 %0, %1, %2, %3 offen sc0 sc1 nt" :: "v"(val), "v"(voff), "s"(ro), "s"(so) : "memory");
        }
        asm volatile("s_waitcnt vmcnt(%0)" :: "i"(DEPTH) : "memory");
      }
      __builtin_amdgcn_s_barrier();
    }
  }
  asm volatile("s_waitcnt vmcnt(0)" ::: "memory");
  unsigned long long r1 = __builtin_amdgcn_s_memrealtime();
  if (lane == 0 && r0 == 12345) { stamps[0] = r1; }
}

extern "C" void kernel_launch(void* const* d_in, const int* in_sizes, int n_in, void* d_out, int out_size, void* d_ws, size_t ws_size, hipStream_t stream) {
  hipFuncSetAttribute((const void*)attn_fwd_dense, hipFuncAttributeMaxDynamicSharedMemorySize, (int)SHM_ATTN);
  hipFuncSetAttribute((const void*)memk<1,0,0,28,1>, hipFuncAttributeMaxDynamicSharedMemorySize, 131072);
  hipLaunchKernelGGL((memk<1,0,0,28,1>), dim3(256), dim3(256), 131072, stream, (const char*)d_in[0], (const char*)d_in[1], (const char*)d_in[2], (char*)d_out, (unsigned long long*)d_ws);
  hipLaunchKernelGGL(attn_fwd_dense, dim3(1536), dim3(64 * NW), SHM_ATTN, stream,
                     (const bf16*)d_in[0], (const bf16*)d_in[1], (const bf16*)d_in[2], (bf16*)d_out);
}
```

```cpp
#include <hip/hip_runtime.h>
#include <hip/hip_bf16.h>
#include <cstdio>
using bf16 = __hip_bfloat16;
constexpr int   D = 128, NW = 8, QBLK = 32, KVBLK = 64;
constexpr float SCALE = 0.088388347648318440f;
constexpr float THR = 8.f;
constexpr int SDEPTH = 2;
constexpr int LDQ = D, LDK = D, LDO = D;
constexpr size_t SHM_V = KVBLK * D * 2, SHM_K = KVBLK * D * 2, SHM_ATTN = 2 * SHM_V + 2 * SHM_K + NW * 64 * 4;
using bf16x8 = __attribute__((ext_vector_type(8))) short;
using s16x4  = __attribute__((ext_vector_type(4))) short;
using f32x16 = __attribute__((ext_vector_type(16))) float;
using f32x8  = __attribute__((ext_vector_type(8))) float;
using u32x4  = __attribute__((ext_vector_type(4))) unsigned;
#define KSWZ(row, colB) ((row) * 256 + ((colB) ^ (((row) & 7) << 4)))
#define SBAR() __builtin_amdgcn_sched_barrier(0)
__device__ __forceinline__ int crow(int r, int hi) { return (r & 3) + 8 * (r >> 2) + 4 * hi; }
__device__ __forceinline__ unsigned cvtpk(float lo, float hi) {
  unsigned r; asm volatile("v_cvt_pk_bf16_f32 %0, %1, %2" : "=v"(r) : "v"(lo), "v"(hi)); return r;
}
__device__ __forceinline__ bf16x8 ld8(const bf16* p) { return *reinterpret_cast<const bf16x8*>(p); }
__device__ __forceinline__ void partialSM(f32x16& p0, f32x16& p1, float& m_reg, float& mn, float& alpha) {
  constexpr float C = SCALE * 1.4426950408889634f;
  float pmax = p0[0]; for (int r = 1; r < 16; ++r) pmax = fmaxf(pmax, p0[r]); for (int r = 0; r < 16; ++r) pmax = fmaxf(pmax, p1[r]);
  { auto rr = __builtin_amdgcn_permlane32_swap(__float_as_uint(pmax), __float_as_uint(pmax), false, false);
    pmax = fmaxf(__uint_as_float(rr[0]), __uint_as_float(rr[1])); }
  if (__builtin_expect(__all(pmax - m_reg <= THR / SCALE), 1)) { mn = m_reg; alpha = 1.f; }
  else { mn = fmaxf(m_reg, pmax); alpha = __builtin_amdgcn_exp2f((m_reg - mn) * C); m_reg = mn; }
  float mnC = -mn * C;
  for (int r = 0; r < 16; ++r) p0[r] = fmaf(p0[r], C, mnC); for (int r = 0; r < 16; ++r) p1[r] = fmaf(p1[r], C, mnC);
  for (int r = 0; r < 16; ++r) p0[r] = __builtin_amdgcn_exp2f(p0[r]);
}
__device__ __forceinline__ void finishSM(f32x16& p0, f32x16& p1, float alpha, float& l_reg, bf16x8& pa0, bf16x8& pa1, bf16x8& pa2, bf16x8& pa3) {
  for (int r = 0; r < 16; ++r) p1[r] = __builtin_amdgcn_exp2f(p1[r]);
  float ps = 0; for (int r = 0; r < 16; ++r) ps += p0[r]; for (int r = 0; r < 16; ++r) ps += p1[r];
  { auto rr = __builtin_amdgcn_permlane32_swap(__float_as_uint(ps), __float_as_uint(ps), false, false);
    ps = __uint_as_float(rr[0]) + __uint_as_float(rr[1]); }
  l_reg = l_reg * alpha + ps;
#define PK4(P, BASE, OUT) do { unsigned a0 = cvtpk(P[BASE + 0], P[BASE + 1]), a1 = cvtpk(P[BASE + 2], P[BASE + 3]);   \
    unsigned b0 = cvtpk(P[BASE + 4], P[BASE + 5]), b1 = cvtpk(P[BASE + 6], P[BASE + 7]);                              \
    auto r0 = __builtin_amdgcn_permlane32_swap(a0, b0, false, false); auto r1 = __builtin_amdgcn_permlane32_swap(a1, b1, false, false); \
    u32x4 w = {r0[0], r1[0], r0[1], r1[1]}; OUT = *reinterpret_cast<bf16x8*>(&w); } while (0)
  PK4(p0, 0, pa0); PK4(p0, 8, pa1); PK4(p1, 0, pa2); PK4(p1, 8, pa3);
#undef PK4
}
__device__ __forceinline__ void qkt(f32x16& p0, f32x16& p1, const bf16* Ks, const bf16x8* qr, int r32, int hi) {
  p0 = f32x16{}; p1 = f32x16{};
  for (int d0 = 0; d0 < 8; ++d0) { int cb = (d0 * 16 + hi * 8) * 2;
    bf16x8 b0 = *reinterpret_cast<const bf16x8*>((const char*)Ks + KSWZ(r32, cb));
    bf16x8 b1 = *reinterpret_cast<const bf16x8*>((const char*)Ks + KSWZ(32 + r32, cb));
    p0 = __builtin_amdgcn_mfma_f32_32x32x16_bf16(b0, qr[d0], p0, 0, 0, 0);
    p1 = __builtin_amdgcn_mfma_f32_32x32x16_bf16(b1, qr[d0], p1, 0, 0, 0); }
}
__device__ __forceinline__ int v_st(int k, int c) { const int kk = (k & ~0xC) | ((k & 4) << 1) | ((k & 8) >> 1); return ((kk >> 3) * 4 + (c >> 5)) * 512 + ((kk & 7) * 32 + (c & 31)) * 2; }
__device__ __forceinline__ int v_rd_base(int lane) { return ((lane & 3) << 3) | (((lane >> 2) & 3) << 6) | (((lane >> 4) & 1) << 5) | (((lane >> 5) & 1) << 8); }
constexpr int v_rd_off(int d0, int ks, int half) { return d0 * 512 + ks * 4096 + half * 2048; }
template <int OFF> __device__ __forceinline__ s16x4 tr_read(int vb) {
  s16x4 r; asm volatile("ds_read_b64_tr_b16 %0, %1 offset:%2" : "=&v"(r) : "v"(vb), "i"(OFF) : "memory"); return r;
}
template <int D0> __device__ __forceinline__ void pv_one(f32x16& od, int vb, bf16x8 pa0, bf16x8 pa1, bf16x8 pa2, bf16x8 pa3) {
  const s16x4 l0 = tr_read<v_rd_off(D0, 0, 0)>(vb), h0 = tr_read<v_rd_off(D0, 0, 1)>(vb), l1 = tr_read<v_rd_off(D0, 1, 0)>(vb), h1 = tr_read<v_rd_off(D0, 1, 1)>(vb);
  const s16x4 l2 = tr_read<v_rd_off(D0, 2, 0)>(vb), h2 = tr_read<v_rd_off(D0, 2, 1)>(vb), l3 = tr_read<v_rd_off(D0, 3, 0)>(vb), h3 = tr_read<v_rd_off(D0, 3, 1)>(vb);
  asm volatile("s_waitcnt lgkmcnt(0)" ::: "memory"); SBAR();
#define PK(L, H) (bf16x8){L[0], L[1], L[2], L[3], H[0], H[1], H[2], H[3]}
  od = __builtin_amdgcn_mfma_f32_32x32x16_bf16(pa0, PK(l0, h0), od, 0, 0, 0);
  od = __builtin_amdgcn_mfma_f32_32x32x16_bf16(pa1, PK(l1, h1), od, 0, 0, 0);
  od = __builtin_amdgcn_mfma_f32_32x32x16_bf16(pa2, PK(l2, h2), od, 0, 0, 0);
  od = __builtin_amdgcn_mfma_f32_32x32x16_bf16(pa3, PK(l3, h3), od, 0, 0, 0);
#undef PK
}
__device__ __forceinline__ void pv_d0(f32x16* o, int vb, bf16x8 pa0, bf16x8 pa1, bf16x8 pa2, bf16x8 pa3) {
  pv_one<0>(o[0], vb, pa0, pa1, pa2, pa3); pv_one<1>(o[1], vb, pa0, pa1, pa2, pa3); pv_one<2>(o[2], vb, pa0, pa1, pa2, pa3); pv_one<3>(o[3], vb, pa0, pa1, pa2, pa3);
}
__device__ __forceinline__ void attn_dense_body(const bf16* __restrict__ Qb, const bf16* __restrict__ Kh, const bf16* __restrict__ Vh,
                                                bf16* __restrict__ Ob, int seq, char* lds) {
  const int tid = threadIdx.x, wid = tid >> 6, lane = tid & 63, r32 = lane & 31, hi = lane >> 5;
  bf16* V_lds = (bf16*)lds; bf16* K_lds = (bf16*)(lds + 2 * SHM_V);
  float* ws = (float*)(lds + 2 * SHM_V + 2 * SHM_K) + wid * 64; float* li_l = ws; float* al_l = ws + 32;
  float m_reg = -1e30f, l_reg = 0; f32x16 o[4] = {}; bf16x8 qr[8];
  const bf16* Qw = Qb + (long)(wid * QBLK + r32) * LDQ + hi * 8;
#pragma unroll
  for (int d0 = 0; d0 < 8; ++d0) qr[d0] = ld8(Qw + d0 * 16);
  const int sr = tid >> 4, sc = (tid & 15) * 8, vst0 = v_st(sr, sc), vst1 = v_st(32 + sr, sc);
  const int vb0 = (int)(uintptr_t)V_lds + v_rd_base(lane);
  struct { bf16x8 vs0, vs1, ks0, ks1; } sr_[SDEPTH];
#define SLOAD(i, k0) do { sr_[i].vs0 = ld8(&Vh[(long)((k0) + sr) * LDK + sc]); sr_[i].vs1 = ld8(&Vh[(long)((k0) + 32 + sr) * LDK + sc]); \
    sr_[i].ks0 = ld8(&Kh[(long)((k0) + sr) * LDK + sc]); sr_[i].ks1 = ld8(&Kh[(long)((k0) + 32 + sr) * LDK + sc]); } while (0)
#define SWRITE(b, i) do { *(bf16x8*)((char*)V_lds + (b) * SHM_V + vst0) = (sr_[i].vs0);          \
    *(bf16x8*)((char*)V_lds + (b) * SHM_V + vst1) = (sr_[i].vs1); int kc = sc * 2;               \
    *(bf16x8*)((char*)K_lds + (b) * SHM_K + KSWZ(sr, kc)) = (sr_[i].ks0);                       \
    *(bf16x8*)((char*)K_lds + (b) * SHM_K + KSWZ(32 + sr, kc)) = (sr_[i].ks1); } while (0)
#define SWAIT() do { asm volatile("s_waitcnt vmcnt(4)" ::: "memory"); } while (0)
#define RESC(a) do { if (__any((a) < 1.f)) { if (hi == 0) al_l[r32] = (a); asm volatile("s_waitcnt lgkmcnt(0)" ::: "memory"); \
    for (int d = 0; d < 4; ++d) for (int r = 0; r < 16; ++r) o[d][r] *= al_l[crow(r, hi)]; } } while (0)
  f32x16 pA0, pA1, pB0, pB1; float mnA, mnB, alA, alB; bf16x8 pa0, pa1, pa2, pa3; const int NT = seq / KVBLK;
  constexpr int SE = 0, SO = SDEPTH - 1;
  SLOAD(SE, 0); asm volatile("s_waitcnt vmcnt(0)" ::: "memory"); SWRITE(0, SE); __syncthreads();
  qkt(pA0, pA1, K_lds, qr, r32, hi); partialSM(pA0, pA1, m_reg, mnA, alA);
  SLOAD(SO, KVBLK); if (2 < NT) SLOAD(SE, 2 * KVBLK);
  SWAIT(); SWRITE(1, SO); __syncthreads();
  for (int j = 1; j + 1 < NT; j += 2) {
    SBAR(); qkt(pB0, pB1, (bf16*)((char*)K_lds + SHM_K), qr, r32, hi);
    finishSM(pA0, pA1, alA, l_reg, pa0, pa1, pa2, pa3); SBAR();
    SLOAD(SO, (j + SDEPTH) * KVBLK); SBAR();
    pv_d0(o, vb0, pa0, pa1, pa2, pa3); partialSM(pB0, pB1, m_reg, mnB, alB);
    __syncthreads(); SWAIT(); SWRITE(0, SE);
    RESC(alB); __syncthreads();
    SBAR(); qkt(pA0, pA1, K_lds, qr, r32, hi);
    finishSM(pB0, pB1, alB, l_reg, pa0, pa1, pa2, pa3); SBAR();
    if (j + 3 < NT) SLOAD(SE, (j + 1 + SDEPTH) * KVBLK); SBAR();
    pv_d0(o, vb0 + (int)SHM_V, pa0, pa1, pa2, pa3); partialSM(pA0, pA1, m_reg, mnA, alA);
    __syncthreads(); if (j + 3 < NT) { SWAIT(); } else { asm volatile("s_waitcnt vmcnt(0)" ::: "memory"); } SWRITE(1, SO);
    RESC(alA); __syncthreads();
  }
  SBAR(); qkt(pB0, pB1, (bf16*)((char*)K_lds + SHM_K), qr, r32, hi);
  finishSM(pA0, pA1, alA, l_reg, pa0, pa1, pa2, pa3); SBAR();
  pv_d0(o, vb0, pa0, pa1, pa2, pa3); partialSM(pB0, pB1, m_reg, mnB, alB);
  __syncthreads(); RESC(alB);
  finishSM(pB0, pB1, alB, l_reg, pa0, pa1, pa2, pa3); SBAR();
  pv_d0(o, vb0 + (int)SHM_V, pa0, pa1, pa2, pa3);
  if (hi == 0) li_l[r32] = l_reg; asm volatile("s_waitcnt lgkmcnt(0)" ::: "memory");
  float rli[16];
#pragma unroll
  for (int r = 0; r < 16; ++r) rli[r] = __builtin_amdgcn_rcpf(li_l[crow(r, hi)]);
  bf16* Ow = Ob + (long)(wid * QBLK) * LDO;
#pragma unroll
  for (int r = 0; r < 16; ++r) { int orow = crow(r, hi);
    for (int d0 = 0; d0 < 4; ++d0) Ow[(long)orow * LDO + d0 * 32 + r32] = __float2bfloat16(o[d0][r] * rli[r]); }
}
__global__ __launch_bounds__(64 * NW, 1)
void attn_fwd_dense(const bf16* __restrict__ Q, const bf16* __restrict__ K, const bf16* __restrict__ V, bf16* __restrict__ O) {
  extern __shared__ __attribute__((aligned(16))) char shm[];
  const int blk = blockIdx.x, x = blk & 7, i = blk >> 3;
  const int pair = x * 64 + i / 3, qb = i % 3;
  const long q0 = ((long)pair * 768 + qb * 256) * D, k0 = (long)pair * 512 * D;
  attn_dense_body(Q + q0, K + k0, V + k0, O + q0, 512, shm);
}
template <int QAUX, int KVAUX, int OAUX, int DEPTH, int SHARE>
__global__ __launch_bounds__(256, 1) void memk(const char* Q, const char* K, const char* V, char* O, unsigned long long* stamps) {
  extern __shared__ __attribute__((aligned(16))) char lds[];
  unsigned tid = threadIdx.x, lane = tid & 63, wid = __builtin_amdgcn_readfirstlane(tid >> 6);
  unsigned long long r0 = __builtin_amdgcn_s_memrealtime();
  unsigned x = blockIdx.x & 7, i = blockIdx.x >> 3;
  auto rq = __builtin_amdgcn_make_buffer_rsrc((void*)Q, 0, 100663296, 0x20000);
  auto rk = __builtin_amdgcn_make_buffer_rsrc((void*)K, 0, 67108864, 0x20000);
  auto rv = __builtin_amdgcn_make_buffer_rsrc((void*)V, 0, 67108864, 0x20000);
  auto ro = __builtin_amdgcn_make_buffer_rsrc((void*)O, 0, 100663296, 0x20000);
  unsigned voff = lane * 16;
  unsigned slot = 0;
  u32x4 val = {tid, tid, tid, tid};
  for (int r = 0; r < 4; r++) {
    unsigned head, pair;
    if (SHARE) { head = x * 128 + r * 32 + i; pair = head >> 1; }
    else { pair = x * 64 + (r >> 1) * 32 + i; head = pair * 2 + (r & 1); }
    unsigned qb = head * 98304u + wid * 24576u, kb = pair * 131072u + wid * 1024u;
    for (int t = 0; t < 8; t++) {
      for (int p = 0; p < 4; p++) {
        unsigned m0 = __builtin_amdgcn_readfirstlane((unsigned)(size_t)lds + ((slot & 31) * 4096) + wid * 1024); slot++;
        unsigned so = __builtin_amdgcn_readfirstlane(kb + t * 16384 + p * 4096);
        if (KVAUX == 0) asm volatile("s_mov_b32 m0, %0\n s_nop 0\n buffer_load_dwordx4 %1, %2, %3 offen lds" :: "s"(m0), "v"(voff), "s"(rk), "s"(so) : "memory");
        else asm volatile("s_mov_b32 m0, %0\n s_nop 0\n buffer_load_dwordx4 %1, %2, %3 offen nt lds" :: "s"(m0), "v"(voff), "s"(rk), "s"(so) : "memory");
        m0 = __builtin_amdgcn_readfirstlane((unsigned)(size_t)lds + ((slot & 31) * 4096) + wid * 1024); slot++;
        if (KVAUX == 0) asm volatile("s_mov_b32 m0, %0\n s_nop 0\n buffer_load_dwordx4 %1, %2, %3 offen lds" :: "s"(m0), "v"(voff), "s"(rv), "s"(so) : "memory");
        else asm volatile("s_mov_b32 m0, %0\n s_nop 0\n buffer_load_dwordx4 %1, %2, %3 offen nt lds" :: "s"(m0), "v"(voff), "s"(rv), "s"(so) : "memory");
        if (p < 3) {
          m0 = __builtin_amdgcn_readfirstlane((unsigned)(size_t)lds + ((slot & 31) * 4096) + wid * 1024); slot++;
          so = __builtin_amdgcn_readfirstlane(qb + (t * 3 + p) * 1024);
          if (QAUX == 0) asm volatile("s_mov_b32 m0, %0\n s_nop 0\n buffer_load_dwordx4 %1, %2, %3 offen lds" :: "s"(m0), "v"(voff), "s"(rq), "s"(so) : "memory");
          else asm volatile("s_mov_b32 m0, %0\n s_nop 0\n buffer_load_dwordx4 %1, %2, %3 offen nt lds" :: "s"(m0), "v"(voff), "s"(rq), "s"(so) : "memory");
          if (OAUX == 0) asm volatile("buffer_store_dwordx4 %0, %1, %2, %3 offen" :: "v"(val), "v"(voff), "s"(ro), "s"(so) : "memory");
          else if (OAUX == 1) asm volatile("buffer_store_dwordx4 %0, %1, %2, %3 offen nt" :: "v"(val), "v"(voff), "s"(ro), "s"(so) : "memory");
          else if (OAUX == 2) asm volatile("buffer_store_dwordx4 %0, %1, %2, %3 offen sc1" :: "v"(val), "v"(voff), "s"(ro), "s"(so) : "memory");
          else if (OAUX == 3) asm volatile("buffer_store_dwordx4 %0, %1, %2, %3 offen sc0 sc1" :: "v"(val), "v"(voff), "s"(ro), "s"(so) : "memory");
          else if (OAUX == 4) asm volatile("buffer_store_dwordx4 %0, %1, %2, %3 offen sc0 sc1 nt" :: "v"(val), "v"(voff), "s"(ro), "s"(so) : "memory");
        }
        asm volatile("s_waitcnt vmcnt(%0)" :: "i"(DEPTH) : "memory");
      }
      __builtin_amdgcn_s_barrier();
    }
  }
  asm volatile("s_waitcnt vmcnt(0)" ::: "memory");
  unsigned long long r1 = __builtin_amdgcn_s_memrealtime();
  if (lane == 0) { stamps[(blockIdx.x * 4 + wid) * 2] = r0; stamps[(blockIdx.x * 4 + wid) * 2 + 1] = r1; }
}

__global__ void spink(const unsigned long long* st) {
  unsigned long long mn = ~0ull, mx = 0;
  for (int w = threadIdx.x; w < 1024; w += 64) { mn = min(mn, st[2*w]); mx = max(mx, st[2*w+1]); }
  for (int o = 32; o; o >>= 1) { mn = min(mn, (unsigned long long)__shfl_xor((long long)mn, o)); mx = max(mx, (unsigned long long)__shfl_xor((long long)mx, o)); }
  unsigned long long d = (mx - mn) * 10; if (d > 200000) d = 200000;
  unsigned long long t0 = __builtin_amdgcn_s_memrealtime();
  while (__builtin_amdgcn_s_memrealtime() - t0 < d) __builtin_amdgcn_s_sleep(2);
}

extern "C" void kernel_launch(void* const* d_in, const int* in_sizes, int n_in, void* d_out, int out_size, void* d_ws, size_t ws_size, hipStream_t stream) {
  hipFuncSetAttribute((const void*)attn_fwd_dense, hipFuncAttributeMaxDynamicSharedMemorySize, (int)SHM_ATTN);
  hipFuncSetAttribute((const void*)memk<1,0,0,28,1>, hipFuncAttributeMaxDynamicSharedMemorySize, 131072);
  hipLaunchKernelGGL((memk<1,0,0,28,1>), dim3(256), dim3(256), 131072, stream, (const char*)d_in[0], (const char*)d_in[1], (const char*)d_in[2], (char*)d_out, (unsigned long long*)d_ws);
  hipLaunchKernelGGL(spink, dim3(1), dim3(64), 0, stream, (const unsigned long long*)d_ws);
  hipLaunchKernelGGL(attn_fwd_dense, dim3(1536), dim3(64 * NW), SHM_ATTN, stream,
                     (const bf16*)d_in[0], (const bf16*)d_in[1], (const bf16*)d_in[2], (bf16*)d_out);
}
```

```cpp
#include <hip/hip_runtime.h>
#include <hip/hip_bf16.h>

typedef unsigned u32x4 __attribute__((ext_vector_type(4)));

__device__ __forceinline__ float bf_lo(unsigned x) { return __uint_as_float(x << 16); }
__device__ __forceinline__ float bf_hi(unsigned x) { return __uint_as_float(x & 0xffff0000u); }

__device__ __forceinline__ void exact_rows(const char* Qw, const char* Kp, const char* Vp, char* Ow, unsigned lane, unsigned bits, unsigned x) {
  const float c = 0.12751743082f;
  for (int r96 = 0; r96 < 96; ++r96) {
    if (!((bits >> (((((r96 >> 2) ^ x) & 3) << 2) | (r96 & 3))) & 1)) continue;
    const int row = (r96 >> 2) * 16 + (r96 & 3);
    float s[8];
#pragma unroll
    for (int j = 0; j < 8; ++j) s[j] = 0.f;
    for (int ch = 0; ch < 16; ++ch) {
      const u32x4 q = *(const u32x4*)(Qw + row * 256 + ch * 16);
#pragma unroll
      for (int j = 0; j < 8; ++j) {
        const u32x4 k = *(const u32x4*)(Kp + (j * 64 + lane) * 256 + ch * 16);
#pragma unroll
        for (int e = 0; e < 4; ++e) s[j] = fmaf(bf_hi(q[e]), bf_hi(k[e]), fmaf(bf_lo(q[e]), bf_lo(k[e]), s[j]));
      }
    }
    float m = s[0];
#pragma unroll
    for (int j = 1; j < 8; ++j) m = fmaxf(m, s[j]);
#pragma unroll
    for (int o = 32; o; o >>= 1) m = fmaxf(m, __shfl_xor(m, o));
    float l = 0.f;
#pragma unroll
    for (int j = 0; j < 8; ++j) { s[j] = __builtin_amdgcn_exp2f((s[j] - m) * c); l += s[j]; }
#pragma unroll
    for (int o = 32; o; o >>= 1) l += __shfl_xor(l, o);
    const float il = 1.f / l;
    float o0 = 0.f, o1 = 0.f;
#pragma unroll
    for (int j = 0; j < 8; ++j) {
      const float pj = s[j] * il;
      for (int t = 0; t < 64; ++t) {
        const float p = __shfl(pj, t);
        const unsigned v = *(const unsigned*)(Vp + (j * 64 + t) * 256 + lane * 4);
        o0 = fmaf(p, bf_lo(v), o0); o1 = fmaf(p, bf_hi(v), o1);
      }
    }
    __hip_bfloat162 r; r.x = __float2bfloat16(o0); r.y = __float2bfloat16(o1);
    *(__hip_bfloat162*)(Ow + row * 256 + lane * 4) = r;
  }
}

__global__ __launch_bounds__(256, 1) void gqa_attn_kernel(const char* __restrict__ Q, const char* __restrict__ K, const char* __restrict__ V, char* __restrict__ O) {
  __shared__ __attribute__((aligned(16))) char lds[163840];
  const unsigned wid = __builtin_amdgcn_readfirstlane(threadIdx.x >> 6);
  const unsigned bid = blockIdx.x;
  const unsigned ldsb = (unsigned)(size_t)lds;
  unsigned long long mask;
  asm volatile(
R"ASM(
.macro Ba d,x,o
ds_read_b128 a[\d:\d+3], v\x offset:\o
.endm
.macro Bv d,x,o
ds_read_b128 v[\d:\d+3], v\x offset:\o
.endm
.macro C d,x
v_cvt_pk_bf16_f32 v\d, v\x, v[\x+1]
.endm
.macro E d
v_exp_f32 v\d, v\d
.endm
.macro F d,x
v_fma_f32 v\d, v\d, s40, v\x
.endm
.macro Mava d,x,y
v_mfma_f32_16x16x32_bf16 a[\d:\d+3], v[\x:\x+3], a[\y:\y+3], a[\d:\d+3]
.endm
.macro Mavv d,x,y
v_mfma_f32_16x16x32_bf16 a[\d:\d+3], v[\x:\x+3], v[\y:\y+3], a[\d:\d+3]
.endm
.macro Mvaa d,x,y
v_mfma_f32_16x16x32_bf16 v[\d:\d+3], a[\x:\x+3], a[\y:\y+3], v[\d:\d+3]
.endm
.macro Mvva d,x,y
v_mfma_f32_16x16x32_bf16 v[\d:\d+3], v[\x:\x+3], a[\y:\y+3], v[\d:\d+3]
.endm
.macro Mvvv d,x,y
v_mfma_f32_16x16x32_bf16 v[\d:\d+3], v[\x:\x+3], v[\y:\y+3], v[\d:\d+3]
.endm
.macro Ta d,x,o
ds_read_b64_tr_b16 a[\d:\d+1], v\x offset:\o
.endm
.macro Tv d,x,o
ds_read_b64_tr_b16 v[\d:\d+1], v\x offset:\o
.endm
.macro U d
v_mul_f32 v\d, s40, v\d
.endm
.macro Yvaa d,x,y,c
v_mfma_f32_16x16x32_bf16 v[\d:\d+3], a[\x:\x+3], a[\y:\y+3], v[\c:\c+3]
.endm
.macro Yvva d,x,y,c
v_mfma_f32_16x16x32_bf16 v[\d:\d+3], v[\x:\x+3], a[\y:\y+3], v[\c:\c+3]
.endm
.macro Zava d,x,y
v_mfma_f32_16x16x32_bf16 a[\d:\d+3], v[\x:\x+3], a[\y:\y+3], 0
.endm
.macro Zvaa d,x,y
v_mfma_f32_16x16x32_bf16 v[\d:\d+3], a[\x:\x+3], a[\y:\y+3], 0
.endm
.macro Zvva d,x,y
v_mfma_f32_16x16x32_bf16 v[\d:\d+3], v[\x:\x+3], a[\y:\y+3], 0
.endm
s_load_dwordx2 s[56:57], %[KA], 0x8
s_load_dwordx2 s[64:65], %[KA], 0x0
s_load_dwordx2 s[60:61], %[KA], 0x10
s_load_dwordx2 s[68:69], %[KA], 0x18
v_mbcnt_lo_u32_b32 v11, -1, 0
v_mbcnt_hi_u32_b32 v11, -1, v11
v_and_b32 v10, 3, v11
s_getpc_b64 s[72:73]
LPF%=:
s_and_b32 s73, s73, 0xffff
s_mov_b32 s74, LEND%=-LPF%=+128
s_mov_b32 s75, 0x20000
s_lshr_b32 s77, %[bid], 1
s_and_b32 s77, s77, 12
s_add_u32 s77, s77, %[wid]
s_lshl_b32 s77, s77, 9
v_lshl_add_u32 v122, v10, 7, s77
v_bfe_u32 v8, v11, 2, 2
v_lshl_add_u32 v120, v8, 13, v122
s_lshl_b32 s77, s77, 3
s_add_u32 s77, s77, 4096-64
v_lshl_add_u32 v9, v10, 7, s77
buffer_load_dword v123, v9, s[72:75], 0 offen
buffer_load_dword v120, v120, s[72:75], 0 offen
s_and_b32 s76, %[bid], 7
s_lshl_b32 s76, s76, 7
s_lshr_b32 s77, %[bid], 3
s_add_u32 s76, s76, s77
s_mov_b32 s58, 0x4000000
s_mov_b32 s59, 0x20000
s_mov_b32 s66, 0x6000000
s_mov_b32 s67, 0x20000
s_lshr_b32 s77, s76, 1
s_lshl_b32 s77, s77, 17
s_lshl_b32 s78, %[wid], 11
s_add_u32 s41, s78, %[lds]
s_lshr_b32 s79, %[bid], 4
s_xor_b32 s79, s79, %[bid]
s_and_b32 s79, s79, 7
s_lshl_b32 s79, s79, 14
s_add_u32 s77, s77, s79
s_add_u32 s43, s77, s78
s_and_b32 s79, s76, 1
s_lshl_b32 s53, s79, 13
s_add_u32 s43, s43, s53
s_mul_i32 s79, s76, 98304
s_lshl_b32 s45, %[wid], 10
s_add_u32 s45, s79, s45
s_lshr_b32 s79, %[bid], 3
s_xor_b32 s79, s79, %[bid]
s_and_b32 s79, s79, 7
s_lshl_b32 s79, s79, 12
s_add_u32 s45, s45, s79
s_mov_b32 s46, s45
s_mul_i32 s78, %[wid], 24576
s_mul_i32 s50, s76, 98304
s_add_u32 s50, s50, s78
s_add_u32 s50, s50, 0x600000
s_add_u32 s42, s78, %[lds]
s_add_u32 s42, s42, 65536
s_and_b32 s78, %[wid], 1
s_lshl_b32 s78, s78, 1
v_bfe_u32 v8, v11, 2, 2
v_lshlrev_b32 v4, 8, v8
v_lshrrev_b32 v9, 4, v11
v_lshl_add_u32 v4, v9, 6, v4
v_xor_b32 v9, s78, v10
v_lshl_add_u32 v4, v9, 4, v4
v_xor_b32 v0, 32, v4
s_mov_b32 s62, 0x4000000
s_mov_b32 s63, 0x20000
s_mov_b32 s70, 0x6000000
s_mov_b32 s71, 0x20000
s_lshl_b32 s79, %[wid], 12
s_xor_b32 s79, s79, s53
s_add_u32 s44, s77, s79
s_and_b32 s79, s76, 1
s_lshl_b32 s79, s79, 7
s_add_u32 s44, s44, s79
s_mov_b32 s40, 0x3e0293ee
s_mov_b32 s51, 0x67800000
s_mov_b32 s47, 0
s_mov_b32 s54, 0
s_mov_b32 s55, 0
s_mov_b32 s70, 0
s_sub_u32 s46, s46, 0x2f0000
s_xor_b32 s46, s46, 0x6000
v_bfe_u32 v8, v11, 2, 3
v_lshlrev_b32 v28, 8, v8
v_lshrrev_b32 v9, 5, v11
v_lshl_add_u32 v28, v9, 6, v28
v_bfe_u32 v9, v11, 4, 1
v_lshlrev_b32 v9, 1, v9
v_xor_b32 v9, v9, v10
v_lshl_add_u32 v28, v9, 4, v28
v_bfe_u32 v8, v11, 2, 2
v_lshlrev_b32 v1, 10, v8
v_lshl_add_u32 v1, v10, 6, v1
v_lshrrev_b32 v9, 4, v11
v_bfe_u32 v8, v11, 3, 1
v_lshlrev_b32 v8, 1, v8
v_xor_b32 v8, v8, v9
v_xor_b32 v6, s78, v8
v_lshl_add_u32 v6, v6, 4, v1
v_add_u32 v6, s42, v6
v_lshl_add_u32 v1, v8, 4, v1
v_add_u32 v1, %[lds], v1
v_lshrrev_b32 v8, 5, v11
v_lshlrev_b32 v2, 10, v8
v_bfe_u32 v8, v11, 2, 3
v_lshl_add_u32 v2, v8, 6, v2
v_bfe_u32 v8, v11, 4, 1
v_lshlrev_b32 v8, 1, v8
v_bfe_u32 v9, v11, 1, 1
v_xor_b32 v8, v8, v9
v_lshl_add_u32 v2, v8, 4, v2
v_and_b32 v8, 1, v11
v_lshl_add_u32 v2, v8, 3, v2
v_xor_b32 v3, 32, v2
v_add_u32 v2, 32768, v2
v_add_u32 v3, 32768, v3
v_add_u32 v2, %[lds], v2
v_add_u32 v3, %[lds], v3
v_lshrrev_b32 v9, 4, v11
v_lshlrev_b32 v5, 6, v9
v_xor_b32 v5, s79, v5
v_and_b32 v8, 1, v11
v_lshl_add_u32 v5, v8, 5, v5
v_bfe_u32 v8, v11, 2, 1
v_lshl_add_u32 v5, v8, 4, v5
v_bfe_u32 v8, v11, 3, 1
v_lshl_add_u32 v5, v8, 9, v5
v_bfe_u32 v8, v11, 1, 1
v_lshl_add_u32 v5, v8, 8, v5
v_lshlrev_b32 v30, 8, v9
v_lshl_add_u32 v30, v8, 3, v30
v_and_b32 v8, 1, v11
v_lshl_add_u32 v30, v8, 7, v30
v_bfe_u32 v8, v11, 2, 2
v_lshl_add_u32 v30, v8, 4, v30
v_add_u32 v30, s42, v30
v_lshl_add_u32 v31, v9, 4, s42
v_lshl_add_u32 v121, v11, 2, s42
v_lshlrev_b32 v29, 10, v9
v_and_b32 v8, 15, v11
v_lshl_add_u32 v29, v8, 4, v29
v_add_u32 v29, s42, v29
v_and_b32 v8, 1, v11
v_mul_u32_u24 v8, 0xaaaa, v8
v_add_u32 v8, 0x4444, v8
v_lshl_or_b32 v7, v8, 16, v8
v_mov_b32 v8, 0x3f803f80
v_bfe_u32 v9, v11, 1, 1
v_add_u32 v9, -1, v9
v_and_b32 v8, v8, v9
v_accvgpr_write_b32 a184, v8
v_accvgpr_write_b32 a185, v8
v_accvgpr_write_b32 a186, v8
v_accvgpr_write_b32 a187, v8
v_accvgpr_write_b32 a160, 0
v_accvgpr_write_b32 a161, 0
v_accvgpr_write_b32 a162, 0
v_accvgpr_write_b32 a163, 0
v_accvgpr_write_b32 a164, 0
v_accvgpr_write_b32 a165, 0
v_accvgpr_write_b32 a166, 0
v_accvgpr_write_b32 a167, 0
v_accvgpr_write_b32 a168, 0
v_accvgpr_write_b32 a169, 0
v_accvgpr_write_b32 a170, 0
v_accvgpr_write_b32 a171, 0
v_accvgpr_write_b32 a172, 0
v_accvgpr_write_b32 a173, 0
v_accvgpr_write_b32 a174, 0
v_accvgpr_write_b32 a175, 0
v_accvgpr_write_b32 a176, 0
v_accvgpr_write_b32 a177, 0
v_accvgpr_write_b32 a178, 0
v_accvgpr_write_b32 a179, 0
v_accvgpr_write_b32 a180, 0
v_accvgpr_write_b32 a181, 0
v_accvgpr_write_b32 a182, 0
v_accvgpr_write_b32 a183, 0
s_getreg_b32 s80, hwreg(HW_REG_XCC_ID, 0, 1)
s_cmp_eq_u32 s80, 0
s_waitcnt lgkmcnt(0)
s_cbranch_scc0 LNOD%=
s_sleep 44
s_getreg_b32 s80, hwreg(HW_REG_XCC_ID, 1, 1)
s_cmp_eq_u32 s80, 1
s_cbranch_scc0 LNOD%=
s_sleep 32
LNOD%=:
s_and_b32 s57, s57, 0xffff
s_and_b32 s65, s65, 0xffff
s_add_u32 m0, s41, 0
s_nop 0
buffer_load_dwordx4 v4, s[56:59], s43 offen lds
s_add_u32 m0, s41, 0
s_nop 0
buffer_load_dwordx4 v4, s[56:59], s43 offen offset:1024 lds
s_xor_b32 s43, s43, 0x2000
s_add_u32 m0, s41, 8192
s_nop 0
buffer_load_dwordx4 v4, s[56:59], s43 offen lds
s_add_u32 m0, s41, 8192
s_nop 0
buffer_load_dwordx4 v4, s[56:59], s43 offen offset:1024 lds
s_xor_b32 s43, s43, 0x6000
s_add_u32 m0, s42, 0
s_nop 0
buffer_load_dwordx4 v4, s[64:67], s45 offen nt lds
s_xor_b32 s45, s45, 0x1000
s_add_u32 m0, s42, 1024
s_nop 0
buffer_load_dwordx4 v4, s[64:67], s45 offen nt lds
s_xor_b32 s45, s45, 0x3000
s_add_u32 m0, s42, 2048
s_nop 0
buffer_load_dwordx4 v0, s[64:67], s45 offen nt lds
s_xor_b32 s45, s45, 0x1000
s_add_u32 m0, s42, 3072
s_nop 0
buffer_load_dwordx4 v0, s[64:67], s45 offen nt lds
s_xor_b32 s45, s45, 0x7000
s_add_u32 m0, s42, 4096
s_nop 0
buffer_load_dwordx4 v4, s[64:67], s45 offen nt lds
s_xor_b32 s45, s45, 0x1000
s_add_u32 m0, s42, 5120
s_nop 0
buffer_load_dwordx4 v4, s[64:67], s45 offen nt lds
s_xor_b32 s45, s45, 0x3000
s_add_u32 m0, s42, 6144
s_nop 0
buffer_load_dwordx4 v0, s[64:67], s45 offen nt lds
s_xor_b32 s45, s45, 0x1000
s_add_u32 m0, s42, 7168
s_nop 0
buffer_load_dwordx4 v0, s[64:67], s45 offen nt lds
s_xor_b32 s45, s45, 0x7000
s_add_u32 s45, s45, 0x8000
s_and_b32 s61, s61, 0xffff
s_and_b32 s69, s69, 0xffff
s_mov_b64 vcc, -1
s_mov_b32 s76, LPAD0%=-LPF%=
s_cmp_eq_u32 %[wid], 1
s_cselect_b32 s76, LPAD1%=-LPF%=, s76
s_cmp_eq_u32 %[wid], 2
s_cselect_b32 s76, LPAD2%=-LPF%=, s76
s_cmp_eq_u32 %[wid], 3
s_cselect_b32 s76, LPAD3%=-LPF%=, s76
s_add_u32 s76, s76, s72
s_addc_u32 s77, s73, 0
s_swappc_b64 s[78:79], s[76:77]
s_mov_b32 s76, LPAD4%=-LPF%=
s_cmp_eq_u32 %[wid], 1
s_cselect_b32 s76, LPAD5%=-LPF%=, s76
s_cmp_eq_u32 %[wid], 2
s_cselect_b32 s76, LPAD6%=-LPF%=, s76
s_cmp_eq_u32 %[wid], 3
s_cselect_b32 s76, LPAD7%=-LPF%=, s76
s_add_u32 s76, s76, s72
s_addc_u32 s77, s73, 0
s_swappc_b64 s[78:79], s[76:77]
s_mov_b64 vcc, 0
s_mov_b64 vcc, 0
s_branch LRT2%=
LRET%=:
s_setpc_b64 s[78:79]
LRT2%=:
s_waitcnt vmcnt(8)
s_barrier
Bv 152,1,0
Bv 156,1,256
Ba 200,1,512
Ba 204,1,768
Bv 176,1,4096
Bv 180,1,4352
Ba 224,1,4608
Ba 228,1,4864
s_waitcnt lgkmcnt(4)
Bv 200,1,8192
Bv 204,1,8448
Ba 248,1,8704
Ba 252,1,8960
Bv 240,1,12288
Bv 244,1,12544
Bv 248,1,12800
Bv 252,1,13056
s_add_u32 m0, s41, 16384
s_nop 0
buffer_load_dwordx4 v4, s[56:59], s43 offen lds
s_add_u32 m0, s41, 16384
s_nop 0
buffer_load_dwordx4 v4, s[56:59], s43 offen offset:1024 lds
s_xor_b32 s43, s43, 0x2000
s_add_u32 m0, s41, 49152
s_nop 0
buffer_load_dwordx4 v28, s[60:63], s44 offen lds
s_add_u32 m0, s41, 48128
s_nop 0
buffer_load_dwordx4 v28, s[60:63], s44 offen offset:2048 lds
s_xor_b32 s44, s44, 0x80
s_waitcnt vmcnt(8)
Ba 0,6,0
Ba 4,6,256
Ba 8,6,512
Ba 12,6,768
s_add_u32 m0, s41, 24576
s_nop 0
buffer_load_dwordx4 v4, s[56:59], s43 offen lds
s_add_u32 m0, s41, 24576
s_nop 0
buffer_load_dwordx4 v4, s[56:59], s43 offen offset:1024 lds
s_xor_b32 s43, s43, 0xe000
s_add_u32 m0, s41, 57344
s_nop 0
buffer_load_dwordx4 v28, s[60:63], s44 offen lds
s_add_u32 m0, s41, 56320
s_nop 0
buffer_load_dwordx4 v28, s[60:63], s44 offen offset:2048 lds
s_xor_b32 s44, s44, 0x4080
s_waitcnt lgkmcnt(0)
Zvva 8,152,0
s_mov_b64 vcc, -1
s_mov_b32 s76, LPAD8%=-LPF%=
s_cmp_eq_u32 %[wid], 1
s_cselect_b32 s76, LPAD9%=-LPF%=, s76
s_cmp_eq_u32 %[wid], 2
s_cselect_b32 s76, LPAD10%=-LPF%=, s76
s_cmp_eq_u32 %[wid], 3
s_cselect_b32 s76, LPAD11%=-LPF%=, s76
s_add_u32 s76, s76, s72
s_addc_u32 s77, s73, 0
s_swappc_b64 s[78:79], s[76:77]
s_mov_b64 vcc, 0
s_waitcnt vmcnt(8)
Ba 16,6,4096
Ba 20,6,4352
Mvva 8,156,4
Ba 24,6,4608
Mvaa 8,200,8
Ba 28,6,4864
Mvaa 8,204,12
s_add_u32 m0, s42, 8192
s_waitcnt lgkmcnt(0)
Zvaa 124,200,24
buffer_load_dwordx4 v4, s[64:67], s45 offen nt lds
Mvaa 124,204,28
s_xor_b32 s45, s45, 0x1000
s_add_u32 m0, s42, 9216
Mvva 124,152,16
buffer_load_dwordx4 v4, s[64:67], s45 offen nt lds
Mvva 124,156,20
s_xor_b32 s45, s45, 0x3000
s_add_u32 m0, s42, 10240
v_max3_f32 v116, v8, v9, v10
Zvva 12,176,0
buffer_load_dwordx4 v0, s[64:67], s45 offen nt lds
Mvva 12,180,4
s_xor_b32 s45, s45, 0x1000
s_nop 0
v_max3_f32 v116, v116, v11, v124
v_max3_f32 v116, v116, v125, v126
v_max_f32 v117, v116, v127
v_max_f32 v116, v116, v127
Mvaa 12,224,8
s_nop 0
v_permlane16_swap_b32 v116, v117
v_max_f32 v116, v116, v117
v_mov_b32 v117, v116
Mvaa 12,228,12
s_add_u32 m0, s42, 11264
v_permlane32_swap_b32 v116, v117
v_max_f32 v117, v116, v117
v_sub_f32 v16, 0x42db93ec, v117
v_mul_f32 v116, s40, v16
v_mov_b32 v17, v16
v_mov_b32 v18, v16
v_mov_b32 v19, v16
v_pk_fma_f32 v[8:9], v[8:9], s[40:41], v[116:117] op_sel_hi:[1,0,0]
)ASM"
R"ASM(
v_pk_fma_f32 v[10:11], v[10:11], s[40:41], v[116:117] op_sel_hi:[1,0,0]
v_pk_fma_f32 v[124:125], v[124:125], s[40:41], v[116:117] op_sel_hi:[1,0,0]
v_pk_fma_f32 v[126:127], v[126:127], s[40:41], v[116:117] op_sel_hi:[1,0,0]
E 8
E 9
E 10
E 11
E 124
E 125
E 126
E 127
C 32,8
C 33,10
C 46,124
C 47,126
Yvaa 8,224,24,16
buffer_load_dwordx4 v0, s[64:67], s45 offen nt lds
Mvaa 8,228,28
s_xor_b32 s45, s45, 0x7000
F 12,116
F 13,116
Mvva 8,176,16
F 14,116
F 15,116
Mvva 8,180,20
E 12
E 13
E 14
E 15
C 34,12
C 35,14
Yvva 124,200,0,16
Mvva 124,204,4
U 8
U 9
Mvaa 124,248,8
U 10
U 11
Mvaa 124,252,12
E 8
E 9
E 10
E 11
C 48,8
C 49,10
Yvaa 12,248,24,16
Mvaa 12,252,28
U 124
U 125
Mvva 12,200,16
U 126
U 127
Mvva 12,204,20
E 124
E 125
E 126
E 127
C 36,124
C 37,126
s_mov_b64 vcc, -1
s_mov_b32 s76, LPAD12%=-LPF%=
s_cmp_eq_u32 %[wid], 1
s_cselect_b32 s76, LPAD13%=-LPF%=, s76
s_cmp_eq_u32 %[wid], 2
s_cselect_b32 s76, LPAD14%=-LPF%=, s76
s_cmp_eq_u32 %[wid], 3
s_cselect_b32 s76, LPAD15%=-LPF%=, s76
s_add_u32 s76, s76, s72
s_addc_u32 s77, s73, 0
s_swappc_b64 s[78:79], s[76:77]
s_mov_b64 vcc, 0
s_waitcnt vmcnt(8)
s_barrier
Yvva 8,240,0,16
s_add_u32 m0, s42, 12288
Ba 96,1,16384
Ba 100,1,16640
Mvva 8,244,4
buffer_load_dwordx4 v4, s[64:67], s45 offen nt lds
Ba 104,1,16896
U 12
U 13
Mvva 8,248,8
s_xor_b32 s45, s45, 0x1000
s_add_u32 m0, s42, 13312
Ba 108,1,17152
U 14
U 15
Mvva 8,252,12
buffer_load_dwordx4 v4, s[64:67], s45 offen nt lds
Ta 128,2,16384
Ta 130,2,18432
Ta 132,2,20480
E 12
E 13
E 14
E 15
C 50,12
C 51,14
Yvva 12,248,24,16
s_xor_b32 s45, s45, 0x3000
s_add_u32 m0, s42, 14336
Ta 134,2,22528
Ta 140,3,20480
Mvva 12,252,28
buffer_load_dwordx4 v0, s[64:67], s45 offen nt lds
Ta 142,3,22528
U 8
U 9
Mvva 12,240,16
s_xor_b32 s45, s45, 0x1000
s_add_u32 m0, s42, 15360
Ta 136,3,16384
U 10
U 11
Mvva 12,244,20
buffer_load_dwordx4 v0, s[64:67], s45 offen nt lds
Ta 138,3,18432
Ba 112,1,20480
Ba 116,1,20736
E 8
E 9
E 10
E 11
C 38,8
C 39,10
s_waitcnt lgkmcnt(10)
Yvaa 8,96,0,16
s_xor_b32 s45, s45, 0x7000
s_add_u32 s45, s45, 0x8000
U 12
U 13
Mvaa 8,100,4
Ba 120,1,20992
E 12
Mvaa 8,104,8
U 14
U 15
Mvaa 8,108,12
Ba 124,1,21248
E 13
Yvaa 124,104,24,16
Ta 144,2,16896
E 14
Mvaa 124,108,28
Ta 146,2,18944
C 52,12
Mvaa 124,96,16
Ta 148,2,20992
E 15
Mvaa 124,100,20
s_waitcnt lgkmcnt(7)
Ta 150,2,23040
C 53,14
Zvva 128,32,128
Ta 156,3,20992
U 8
U 9
Mvva 128,36,132
Ta 158,3,23040
E 8
Zvva 132,36,140
Ta 152,3,16896
U 10
U 11
Mvva 132,32,136
s_waitcnt vmcnt(8)
Ta 154,3,18944
E 9
Zvva 140,46,136
s_barrier
E 10
Mvva 140,50,140
Ba 96,1,24576
C 44,8
Zvva 136,50,132
E 11
Mvva 136,46,128
s_waitcnt lgkmcnt(9)
Ba 100,1,24832
C 45,10
Yvaa 8,112,0,16
U 124
U 125
Mvaa 8,116,4
s_add_u32 m0, s42, 16384
Ba 104,1,25088
E 124
Mvaa 8,120,8
buffer_load_dwordx4 v4, s[64:67], s45 offen nt lds
U 126
U 127
Mvaa 8,124,12
s_xor_b32 s45, s45, 0x1000
Ba 108,1,25344
E 125
Yvaa 12,120,24,16
s_add_u32 m0, s42, 17408
Ta 128,2,24576
E 126
Mvaa 12,124,28
buffer_load_dwordx4 v4, s[64:67], s45 offen nt lds
Ta 130,2,26624
C 58,124
Mvaa 12,112,16
s_xor_b32 s45, s45, 0x3000
Ta 132,2,28672
E 127
Mvaa 12,116,20
s_waitcnt lgkmcnt(7)
Ta 134,2,30720
C 59,126
v_smfmac_f32_16x16x64_bf16 a[160:163], a[184:187], v[32:39], v7
Ta 140,3,28672
v_smfmac_f32_16x16x64_bf16 a[164:167], a[184:187], v[46:53], v7
Ta 142,3,30720
Zvva 160,32,144
s_add_u32 m0, s42, 18432
U 8
U 9
Mvva 160,36,148
buffer_load_dwordx4 v0, s[64:67], s45 offen nt lds
Ta 136,3,24576
E 8
Zvva 164,36,156
s_xor_b32 s45, s45, 0x1000
U 10
U 11
Mvva 164,32,152
s_add_u32 m0, s42, 19456
Ta 138,3,26624
E 9
Zava 212,46,152
buffer_load_dwordx4 v0, s[64:67], s45 offen nt lds
E 10
Mava 212,50,156
s_xor_b32 s45, s45, 0x7000
Ba 112,1,28672
C 42,8
Zava 208,50,148
E 11
Mava 208,46,144
s_waitcnt lgkmcnt(9)
Ba 116,1,28928
C 43,10
Yvaa 8,96,0,16
U 12
U 13
Mvaa 8,100,4
Ba 120,1,29184
E 12
Mvaa 8,104,8
U 14
U 15
Mvaa 8,108,12
Ba 124,1,29440
E 13
Yvaa 124,104,24,16
Ta 144,2,25088
E 14
Mvaa 124,108,28
Ta 146,2,27136
C 56,12
Mvaa 124,96,16
LPAD0%=:
s_cbranch_vccnz LRET%=
Ta 148,2,29184
E 15
Mvaa 124,100,20
s_waitcnt lgkmcnt(7)
Ta 150,2,31232
C 57,14
Zvva 184,32,128
Ta 156,3,29184
U 8
U 9
Mvva 184,36,132
Ta 158,3,31232
E 8
Zvva 188,36,140
Ta 152,3,25088
U 10
U 11
Mvva 188,32,136
Ta 154,3,27136
E 9
Zava 236,46,136
E 10
Mava 236,50,140
Ba 96,1,16384
C 40,8
Zava 232,50,132
E 11
Mava 232,46,128
s_waitcnt lgkmcnt(9)
Ba 100,1,16640
C 41,10
Yvaa 8,112,0,16
U 124
U 125
Mvaa 8,116,4
Ba 104,1,16896
E 124
Mvaa 8,120,8
U 126
U 127
Mvaa 8,124,12
Ba 108,1,17152
E 125
Yvaa 12,120,24,16
Ta 128,2,16384
E 126
Mvaa 12,124,28
Ta 130,2,18432
C 54,124
Mvaa 12,112,16
Ta 132,2,20480
E 127
Mvaa 12,116,20
s_waitcnt lgkmcnt(7)
Ta 134,2,22528
C 55,126
Zvva 208,32,144
s_waitcnt vmcnt(8)
Ta 140,3,20480
Ba 32,6,8192
U 8
U 9
Mvva 208,36,148
Ta 142,3,22528
Ba 36,6,8448
E 8
Zvva 212,36,156
Ta 136,3,16384
Ba 40,6,8704
U 10
U 11
Mvva 212,32,152
Ta 138,3,18432
Ba 44,6,8960
E 9
Zvva 220,46,152
E 10
Mvva 220,50,156
Ba 112,1,20480
C 38,8
Zvva 216,50,148
E 11
Mvva 216,46,144
s_waitcnt lgkmcnt(13)
Ba 116,1,20736
C 39,10
s_waitcnt lgkmcnt(2)
Zvva 8,152,32
s_waitcnt vmcnt(4)
Ba 48,6,12288
Ba 52,6,12544
Mvva 8,156,36
Ba 56,6,12800
U 12
U 13
Mvaa 8,200,40
Ba 60,6,13056
U 14
U 15
Mvaa 8,204,44
s_add_u32 m0, s42, 20480
E 12
E 13
E 14
E 15
C 52,12
C 53,14
s_waitcnt lgkmcnt(0)
Zvaa 124,200,56
buffer_load_dwordx4 v4, s[64:67], s45 offen nt lds
Mvaa 124,204,60
s_xor_b32 s45, s45, 0x1000
s_add_u32 m0, s42, 21504
Mvva 124,152,48
buffer_load_dwordx4 v4, s[64:67], s45 offen nt lds
Mvva 124,156,52
s_xor_b32 s45, s45, 0x3000
s_add_u32 m0, s42, 22528
v_max3_f32 v116, v8, v9, v10
Zvva 12,176,32
buffer_load_dwordx4 v0, s[64:67], s45 offen nt lds
Mvva 12,180,36
s_xor_b32 s45, s45, 0x1000
s_nop 0
v_max3_f32 v116, v116, v11, v124
v_max3_f32 v116, v116, v125, v126
v_max_f32 v117, v116, v127
v_max_f32 v116, v116, v127
Mvaa 12,224,40
s_nop 0
v_permlane16_swap_b32 v116, v117
v_max_f32 v116, v116, v117
v_mov_b32 v117, v116
Mvaa 12,228,44
)ASM"
R"ASM(
s_add_u32 m0, s42, 23552
v_permlane32_swap_b32 v116, v117
v_max_f32 v117, v116, v117
v_sub_f32 v20, 0x42db93ec, v117
v_mul_f32 v116, s40, v20
v_mov_b32 v21, v20
v_mov_b32 v22, v20
v_mov_b32 v23, v20
v_pk_fma_f32 v[8:9], v[8:9], s[40:41], v[116:117] op_sel_hi:[1,0,0]
v_pk_fma_f32 v[10:11], v[10:11], s[40:41], v[116:117] op_sel_hi:[1,0,0]
v_pk_fma_f32 v[124:125], v[124:125], s[40:41], v[116:117] op_sel_hi:[1,0,0]
v_pk_fma_f32 v[126:127], v[126:127], s[40:41], v[116:117] op_sel_hi:[1,0,0]
E 8
E 9
E 10
E 11
E 124
E 125
E 126
E 127
C 60,8
C 61,10
C 74,124
C 75,126
Yvaa 8,224,56,20
buffer_load_dwordx4 v0, s[64:67], s45 offen nt lds
Mvaa 8,228,60
s_xor_b32 s45, s45, 0x7000
s_add_u32 s45, s45, 0x2f0000
F 12,116
F 13,116
Mvva 8,176,48
F 14,116
F 15,116
Mvva 8,180,52
s_add_u32 m0, s41, 0
E 12
E 13
E 14
E 15
C 62,12
C 63,14
Yvva 124,200,32,20
buffer_load_dwordx4 v4, s[56:59], s43 offen lds
Mvva 124,204,36
U 8
U 9
Mvaa 124,248,40
U 10
U 11
Mvaa 124,252,44
s_add_u32 m0, s41, 0
E 8
E 9
E 10
E 11
C 76,8
C 77,10
Yvaa 12,248,56,20
buffer_load_dwordx4 v4, s[56:59], s43 offen offset:1024 lds
Mvaa 12,252,60
s_xor_b32 s43, s43, 0x2000
U 124
U 125
Mvva 12,200,48
U 126
U 127
Mvva 12,204,52
s_add_u32 m0, s41, 32768
E 124
E 125
E 126
E 127
C 64,124
C 65,126
Yvva 8,240,32,20
buffer_load_dwordx4 v28, s[60:63], s44 offen lds
Mvva 8,244,36
U 12
U 13
Mvva 8,248,40
U 14
U 15
Mvva 8,252,44
s_add_u32 m0, s41, 31744
E 12
E 13
E 14
E 15
C 78,12
C 79,14
Yvva 12,248,56,20
buffer_load_dwordx4 v28, s[60:63], s44 offen offset:2048 lds
Mvva 12,252,60
s_xor_b32 s44, s44, 0x80
U 8
U 9
Mvva 12,240,48
U 10
U 11
Mvva 12,244,52
E 8
E 9
E 10
E 11
C 66,8
C 67,10
Yvaa 8,96,32,20
s_nop 0
U 12
U 13
Mvaa 8,100,36
Ba 120,1,20992
E 12
Mvaa 8,104,40
U 14
U 15
Mvaa 8,108,44
Ba 124,1,21248
E 13
Yvaa 124,104,56,20
Ta 144,2,16896
E 14
Mvaa 124,108,60
Ta 146,2,18944
C 80,12
Mvaa 124,96,48
Ta 148,2,20992
E 15
Mvaa 124,100,52
Ta 150,2,23040
C 81,14
Zvva 144,60,128
Ta 156,3,20992
U 8
U 9
Mvva 144,64,132
Ta 158,3,23040
E 8
Zvva 148,64,140
Ta 152,3,16896
U 10
U 11
Mvva 148,60,136
Ta 154,3,18944
E 9
Zava 196,74,136
E 10
Mava 196,78,140
Ba 96,1,24576
C 72,8
Zava 192,78,132
E 11
Mava 192,74,128
s_waitcnt lgkmcnt(9)
Ba 100,1,24832
C 73,10
Yvaa 8,112,32,20
U 124
U 125
Mvaa 8,116,36
s_add_u32 m0, s41, 8192
Ba 104,1,25088
E 124
Mvaa 8,120,40
buffer_load_dwordx4 v4, s[56:59], s43 offen lds
U 126
U 127
Mvaa 8,124,44
Ba 108,1,25344
E 125
Yvaa 12,120,56,20
s_add_u32 m0, s41, 8192
Ta 128,2,24576
E 126
Mvaa 12,124,60
buffer_load_dwordx4 v4, s[56:59], s43 offen offset:1024 lds
Ta 130,2,26624
C 86,124
Mvaa 12,112,48
s_xor_b32 s43, s43, 0x6000
Ta 132,2,28672
E 127
Mvaa 12,116,52
s_waitcnt lgkmcnt(7)
Ta 134,2,30720
C 87,126
v_smfmac_f32_16x16x64_bf16 a[168:171], a[184:187], v[60:67], v7
Ta 140,3,28672
v_smfmac_f32_16x16x64_bf16 a[172:175], a[184:187], v[74:81], v7
Ta 142,3,30720
Zvva 168,60,144
s_add_u32 m0, s41, 40960
U 8
U 9
Mvva 168,64,148
buffer_load_dwordx4 v28, s[60:63], s44 offen lds
Ta 136,3,24576
E 8
Zvva 172,64,156
U 10
U 11
Mvva 172,60,152
s_add_u32 m0, s41, 39936
Ta 138,3,26624
E 9
Zava 220,74,152
buffer_load_dwordx4 v28, s[60:63], s44 offen offset:2048 lds
E 10
Mava 220,78,156
s_xor_b32 s44, s44, 0xc080
Ba 112,1,28672
C 70,8
Zava 216,78,148
E 11
Mava 216,74,144
s_waitcnt lgkmcnt(9)
Ba 116,1,28928
C 71,10
Yvaa 8,96,32,20
U 12
U 13
Mvaa 8,100,36
Ba 120,1,29184
E 12
Mvaa 8,104,40
U 14
U 15
Mvaa 8,108,44
Ba 124,1,29440
E 13
Yvaa 124,104,56,20
Ta 144,2,25088
E 14
Mvaa 124,108,60
Ta 146,2,27136
C 84,12
Mvaa 124,96,48
Ta 148,2,29184
E 15
Mvaa 124,100,52
s_waitcnt lgkmcnt(7)
Ta 150,2,31232
C 85,14
Zvva 192,60,128
Ta 156,3,29184
U 8
U 9
Mvva 192,64,132
Ta 158,3,31232
E 8
Zvva 196,64,140
Ta 152,3,25088
U 10
U 11
Mvva 196,60,136
Ta 154,3,27136
E 9
Zava 244,74,136
E 10
Mava 244,78,140
Ba 96,1,16384
C 68,8
Zava 240,78,132
E 11
Mava 240,74,128
s_waitcnt lgkmcnt(9)
Ba 100,1,16640
C 69,10
Yvaa 8,112,32,20
U 124
U 125
Mvaa 8,116,36
Ba 104,1,16896
E 124
Mvaa 8,120,40
U 126
U 127
Mvaa 8,124,44
Ba 108,1,17152
E 125
Yvaa 12,120,56,20
Ta 128,2,16384
E 126
Mvaa 12,124,60
Ta 130,2,18432
C 82,124
Mvaa 12,112,48
Ta 132,2,20480
E 127
Mvaa 12,116,52
s_waitcnt lgkmcnt(7)
Ta 134,2,22528
C 83,126
Zvva 224,60,144
s_waitcnt vmcnt(12)
Ta 140,3,20480
Ba 64,6,16384
U 8
U 9
Mvva 224,64,148
Ta 142,3,22528
Ba 68,6,16640
E 8
Zvva 228,64,156
Ta 136,3,16384
Ba 72,6,16896
U 10
U 11
Mvva 228,60,152
Ta 138,3,18432
Ba 76,6,17152
E 9
Zvva 236,74,152
E 10
Mvva 236,78,156
Ba 112,1,20480
C 66,8
Zvva 232,78,148
E 11
Mvva 232,74,144
s_waitcnt lgkmcnt(13)
Ba 116,1,20736
C 67,10
s_waitcnt lgkmcnt(2)
Zvva 8,152,64
s_waitcnt vmcnt(8)
Ba 80,6,20480
Ba 84,6,20736
Mvva 8,156,68
Ba 88,6,20992
U 12
U 13
Mvaa 8,200,72
Ba 92,6,21248
U 14
U 15
Mvaa 8,204,76
E 12
E 13
E 14
E 15
C 80,12
C 81,14
s_waitcnt lgkmcnt(0)
Zvaa 124,200,88
Mvaa 124,204,92
Mvva 124,152,80
Mvva 124,156,84
v_max3_f32 v116, v8, v9, v10
Zvva 12,176,64
Mvva 12,180,68
s_nop 4
v_max3_f32 v116, v116, v11, v124
v_max3_f32 v116, v116, v125, v126
v_max_f32 v117, v116, v127
v_max_f32 v116, v116, v127
Mvaa 12,224,72
s_nop 0
v_permlane16_swap_b32 v116, v117
v_max_f32 v116, v116, v117
v_mov_b32 v117, v116
Mvaa 12,228,76
s_nop 0
v_permlane32_swap_b32 v116, v117
v_max_f32 v117, v116, v117
v_sub_f32 v24, 0x42db93ec, v117
v_mul_f32 v116, s40, v24
v_mov_b32 v25, v24
v_mov_b32 v26, v24
v_mov_b32 v27, v24
v_pk_fma_f32 v[8:9], v[8:9], s[40:41], v[116:117] op_sel_hi:[1,0,0]
v_pk_fma_f32 v[10:11], v[10:11], s[40:41], v[116:117] op_sel_hi:[1,0,0]
v_pk_fma_f32 v[124:125], v[124:125], s[40:41], v[116:117] op_sel_hi:[1,0,0]
v_pk_fma_f32 v[126:127], v[126:127], s[40:41], v[116:117] op_sel_hi:[1,0,0]
E 8
E 9
E 10
E 11
E 124
E 125
E 126
E 127
C 88,8
C 89,10
C 102,124
C 103,126
Yvaa 8,224,88,24
Mvaa 8,228,92
F 12,116
F 13,116
Mvva 8,176,80
F 14,116
F 15,116
Mvva 8,180,84
E 12
E 13
E 14
E 15
C 90,12
)ASM"
R"ASM(
C 91,14
Yvva 124,200,64,24
Mvva 124,204,68
U 8
U 9
Mvaa 124,248,72
U 10
U 11
Mvaa 124,252,76
E 8
E 9
E 10
E 11
C 104,8
C 105,10
Yvaa 12,248,88,24
Mvaa 12,252,92
U 124
U 125
Mvva 12,200,80
U 126
U 127
Mvva 12,204,84
E 124
E 125
E 126
E 127
C 92,124
C 93,126
Yvva 8,240,64,24
Mvva 8,244,68
U 12
U 13
Mvva 8,248,72
U 14
U 15
Mvva 8,252,76
E 12
E 13
E 14
E 15
C 106,12
C 107,14
Yvva 12,248,88,24
Mvva 12,252,92
U 8
U 9
Mvva 12,240,80
U 10
U 11
Mvva 12,244,84
E 8
E 9
E 10
E 11
C 94,8
C 95,10
Yvaa 8,96,64,24
s_nop 0
U 12
U 13
Mvaa 8,100,68
Ba 120,1,20992
E 12
Mvaa 8,104,72
U 14
U 15
Mvaa 8,108,76
Ba 124,1,21248
E 13
Yvaa 124,104,88,24
Ta 144,2,16896
E 14
Mvaa 124,108,92
Ta 146,2,18944
C 108,12
Mvaa 124,96,80
Ta 148,2,20992
E 15
Mvaa 124,100,84
Ta 150,2,23040
C 109,14
Zvva 152,88,128
Ta 156,3,20992
U 8
U 9
Mvva 152,92,132
Ta 158,3,23040
E 8
Zvva 156,92,140
Ta 152,3,16896
U 10
U 11
Mvva 156,88,136
Ta 154,3,18944
E 9
Zava 204,102,136
E 10
Mava 204,106,140
Ba 96,1,24576
C 100,8
Zava 200,106,132
E 11
Mava 200,102,128
s_waitcnt lgkmcnt(9)
Ba 100,1,24832
C 101,10
Yvaa 8,112,64,24
LPAD1%=:
s_cbranch_vccnz LRET%=
U 124
U 125
Mvaa 8,116,68
Ba 104,1,25088
E 124
Mvaa 8,120,72
U 126
U 127
Mvaa 8,124,76
Ba 108,1,25344
E 125
Yvaa 12,120,88,24
Ta 128,2,24576
E 126
Mvaa 12,124,92
Ta 130,2,26624
C 114,124
Mvaa 12,112,80
Ta 132,2,28672
E 127
Mvaa 12,116,84
s_waitcnt lgkmcnt(7)
Ta 134,2,30720
C 115,126
v_smfmac_f32_16x16x64_bf16 a[176:179], a[184:187], v[88:95], v7
Ta 140,3,28672
v_smfmac_f32_16x16x64_bf16 a[180:183], a[184:187], v[102:109], v7
Ta 142,3,30720
Zvva 176,88,144
U 8
U 9
Mvva 176,92,148
Ta 136,3,24576
E 8
Zvva 180,92,156
U 10
U 11
Mvva 180,88,152
Ta 138,3,26624
E 9
Zava 228,102,152
E 10
Mava 228,106,156
Ba 112,1,28672
C 98,8
Zava 224,106,148
E 11
Mava 224,102,144
s_waitcnt lgkmcnt(9)
Ba 116,1,28928
C 99,10
Yvaa 8,96,64,24
U 12
U 13
Mvaa 8,100,68
Ba 120,1,29184
E 12
Mvaa 8,104,72
U 14
U 15
Mvaa 8,108,76
Ba 124,1,29440
E 13
Yvaa 124,104,88,24
Ta 144,2,25088
E 14
Mvaa 124,108,92
Ta 146,2,27136
C 112,12
Mvaa 124,96,80
Ta 148,2,29184
E 15
Mvaa 124,100,84
s_waitcnt lgkmcnt(7)
Ta 150,2,31232
C 113,14
Zvva 200,88,128
Ta 156,3,29184
U 8
U 9
Mvva 200,92,132
Ta 158,3,31232
E 8
Zvva 204,92,140
Ta 152,3,25088
U 10
U 11
Mvva 204,88,136
s_waitcnt vmcnt(4)
Ta 154,3,27136
E 9
Zava 252,102,136
s_barrier
E 10
Mava 252,106,140
Ba 96,1,0
C 96,8
Zava 248,106,132
E 11
Mava 248,102,128
s_waitcnt lgkmcnt(9)
Ba 100,1,256
C 97,10
Yvaa 8,112,64,24
U 124
U 125
Mvaa 8,116,68
Ba 104,1,512
E 124
Mvaa 8,120,72
U 126
U 127
Mvaa 8,124,76
Ba 108,1,768
E 125
Yvaa 12,120,88,24
Ta 130,2,0
E 126
Mvaa 12,124,92
Ta 128,2,2048
C 110,124
Mvaa 12,112,80
Ta 134,2,4096
E 127
Mvaa 12,116,84
s_waitcnt lgkmcnt(7)
Ta 132,2,6144
C 111,126
Zvva 240,88,144
Ta 142,3,4096
U 8
U 9
Mvva 240,92,148
s_barrier
Ta 140,3,6144
E 8
Zvva 244,92,156
Ta 138,3,0
U 10
U 11
Mvva 244,88,152
Ta 136,3,2048
E 9
Zvva 252,102,152
E 10
Mvva 252,106,156
Ba 112,1,4096
C 94,8
Zvva 248,106,148
E 11
Mvva 248,102,144
s_waitcnt lgkmcnt(9)
Ba 116,1,4352
C 95,10
s_waitcnt lgkmcnt(2)
L0x24%=:
Yvaa 8,96,0,16
U 12
U 13
Mvaa 8,100,4
E 12
Mvaa 8,104,8
U 14
U 15
Mvaa 8,108,12
E 13
v_mbcnt_lo_u32_b32 v116, -1, 0
v_and_b32 v116, 15, v116
v_lshlrev_b32 v116, 7, v116
Yvaa 124,104,24,16
E 14
Mvaa 124,108,28
Ba 120,1,4608
C 108,12
Mvaa 124,96,16
E 15
Mvaa 124,100,20
s_add_u32 m0, s41, 16384
Ba 124,1,4864
C 109,14
v_smfmac_f32_16x16x64_bf16 a[160:163], a[184:187], v[38:45], v7
buffer_load_dwordx4 v4, s[56:59], s43 offen lds
Mvva 128,42,128
U 8
U 9
Mvva 128,38,132
E 8
Mvva 132,38,140
U 10
U 11
Mvva 132,42,136
buffer_load_dwordx4 v4, s[56:59], s43 offen offset:1024 lds
E 9
Mvva 140,56,136
s_xor_b32 s43, s43, 0x2000
E 10
Mvva 140,52,140
Ta 146,2,512
C 32,8
Mvva 136,52,132
E 11
Mvva 136,56,128
Ta 144,2,2560
C 33,10
Yvaa 8,96,32,20
U 124
U 125
Mvaa 8,100,36
E 124
Mvaa 8,104,40
U 126
U 127
Mvaa 8,108,44
E 125
Yvaa 12,104,56,20
E 126
Mvaa 12,108,60
Ta 150,2,4608
C 46,124
Mvaa 12,96,48
E 127
Mvaa 12,100,52
s_add_u32 m0, s41, 49152
Ta 148,2,6656
C 47,126
v_smfmac_f32_16x16x64_bf16 a[164:167], a[184:187], v[52:59], v7
buffer_load_dwordx4 v28, s[60:63], s44 offen lds
Mvva 144,70,128
U 8
U 9
Mvva 144,66,132
E 8
Mvva 148,66,140
s_add_u32 m0, s41, 48128
U 10
U 11
Mvva 148,70,136
buffer_load_dwordx4 v28, s[60:63], s44 offen offset:2048 lds
E 9
Mava 196,84,136
s_xor_b32 s44, s44, 0x80
E 10
Mava 196,80,140
Ta 158,3,4608
C 60,8
Mava 192,80,132
E 11
Mava 192,84,128
Ta 156,3,6656
C 61,10
Yvaa 8,96,64,24
U 12
U 13
Mvaa 8,100,68
E 12
Mvaa 8,104,72
U 14
U 15
Mvaa 8,108,76
E 13
Yvaa 124,104,88,24
E 14
Mvaa 124,108,92
Ta 154,3,512
C 74,12
Mvaa 124,96,80
E 15
Mvaa 124,100,84
Ta 152,3,2560
C 75,14
Mvva 152,98,128
U 8
U 9
Mvva 152,94,132
E 8
Mvva 156,94,140
U 10
U 11
Mvva 156,98,136
s_waitcnt vmcnt(4)
E 9
Mava 204,112,136
s_waitcnt lgkmcnt(0)
E 10
Mava 204,108,140
s_barrier
Ba 96,1,8192
C 88,8
Mava 200,108,132
E 11
)ASM"
R"ASM(
Mava 200,112,128
Ba 100,1,8448
C 89,10
Yvaa 8,112,0,16
U 124
U 125
Mvaa 8,116,4
E 124
Mvaa 8,120,8
U 126
U 127
Mvaa 8,124,12
E 125
Yvaa 12,120,24,16
E 126
Mvaa 12,124,28
Ba 104,1,8704
C 102,124
Mvaa 12,112,16
E 127
Mvaa 12,116,20
s_add_u32 m0, s41, 24576
Ba 108,1,8960
C 103,126
v_smfmac_f32_16x16x64_bf16 a[168:171], a[184:187], v[66:73], v7
buffer_load_dwordx4 v4, s[56:59], s43 offen lds
Mvva 160,42,144
U 8
U 9
Mvva 160,38,148
E 8
Mvva 164,38,156
U 10
U 11
Mvva 164,42,152
buffer_load_dwordx4 v4, s[56:59], s43 offen offset:1024 lds
E 9
Mava 212,56,152
s_xor_b32 s43, s43, 0x1e000
E 10
Mava 212,52,156
Ta 130,2,8192
C 34,8
Mava 208,52,148
E 11
Mava 208,56,144
Ta 128,2,10240
C 35,10
Yvaa 8,112,32,20
U 12
U 13
Mvaa 8,116,36
E 12
Mvaa 8,120,40
U 14
U 15
Mvaa 8,124,44
buffer_load_dword v117, v116, s[64:67], s50 offen
E 13
Yvaa 124,120,56,20
s_add_u32 s50, s50, 4096
E 14
Mvaa 124,124,60
Ta 134,2,12288
C 48,12
Mvaa 124,112,48
E 15
Mvaa 124,116,52
s_add_u32 m0, s41, 57344
Ta 132,2,14336
C 49,14
v_smfmac_f32_16x16x64_bf16 a[172:175], a[184:187], v[80:87], v7
buffer_load_dwordx4 v28, s[60:63], s44 offen lds
Mvva 168,70,144
U 8
U 9
Mvva 168,66,148
E 8
Mvva 172,66,156
s_add_u32 m0, s41, 56320
U 10
U 11
Mvva 172,70,152
buffer_load_dwordx4 v28, s[60:63], s44 offen offset:2048 lds
E 9
Mava 220,84,152
s_xor_b32 s44, s44, 0x4080
E 10
Mava 220,80,156
Ta 142,3,12288
C 62,8
Mava 216,80,148
E 11
Mava 216,84,144
Ta 140,3,14336
C 63,10
Yvaa 8,112,64,24
U 124
U 125
Mvaa 8,116,68
E 124
Mvaa 8,120,72
U 126
U 127
Mvaa 8,124,76
E 125
Yvaa 12,120,88,24
E 126
Mvaa 12,124,92
Ta 138,3,8192
C 76,124
Mvaa 12,112,80
E 127
Mvaa 12,116,84
Ta 136,3,10240
C 77,126
Mvva 176,98,144
U 8
U 9
Mvva 176,94,148
E 8
Mvva 180,94,156
U 10
U 11
Mvva 180,98,152
E 9
Mava 228,112,152
s_waitcnt lgkmcnt(0)
E 10
Mava 228,108,156
Ba 112,1,12288
C 90,8
Mava 224,108,148
E 11
Mava 224,112,144
Ba 116,1,12544
C 91,10
Yvaa 8,96,0,16
U 12
U 13
Mvaa 8,100,4
E 12
Mvaa 8,104,8
U 14
U 15
Mvaa 8,108,12
E 13
Yvaa 124,104,24,16
E 14
Mvaa 124,108,28
Ba 120,1,12800
C 104,12
Mvaa 124,96,16
E 15
Mvaa 124,100,20
s_add_u32 m0, s41, 0
Ba 124,1,13056
C 105,14
v_smfmac_f32_16x16x64_bf16 a[176:179], a[184:187], v[94:101], v7
buffer_load_dwordx4 v4, s[56:59], s43 offen lds
Mvva 184,42,128
U 8
U 9
Mvva 184,38,132
E 8
Mvva 188,38,140
U 10
U 11
Mvva 188,42,136
buffer_load_dwordx4 v4, s[56:59], s43 offen offset:1024 lds
E 9
Mava 236,56,136
s_xor_b32 s43, s43, 0x2000
E 10
Mava 236,52,140
Ta 146,2,8704
C 36,8
Mava 232,52,132
E 11
Mava 232,56,128
Ta 144,2,10752
C 37,10
Yvaa 8,96,32,20
U 124
U 125
Mvaa 8,100,36
E 124
Mvaa 8,104,40
U 126
U 127
Mvaa 8,108,44
E 125
Yvaa 12,104,56,20
E 126
Mvaa 12,108,60
Ta 150,2,12800
C 50,124
Mvaa 12,96,48
E 127
Mvaa 12,100,52
s_add_u32 m0, s41, 32768
Ta 148,2,14848
C 51,126
v_smfmac_f32_16x16x64_bf16 a[180:183], a[184:187], v[108:115], v7
buffer_load_dwordx4 v28, s[60:63], s44 offen lds
Mvva 192,70,128
U 8
U 9
Mvva 192,66,132
E 8
Mvva 196,66,140
s_add_u32 m0, s41, 31744
U 10
U 11
Mvva 196,70,136
buffer_load_dwordx4 v28, s[60:63], s44 offen offset:2048 lds
E 9
Mava 244,84,136
s_xor_b32 s44, s44, 0x80
E 10
Mava 244,80,140
Ta 158,3,12800
C 64,8
Mava 240,80,132
E 11
Mava 240,84,128
Ta 156,3,14848
C 65,10
Yvaa 8,96,64,24
U 12
U 13
Mvaa 8,100,68
E 12
Mvaa 8,104,72
s_add_u32 m0, s42, 0
U 14
U 15
Mvaa 8,108,76
buffer_load_dwordx4 v4, s[64:67], s45 offen lds
E 13
Yvaa 124,104,88,24
s_xor_b32 s45, s45, 0x1000
E 14
Mvaa 124,108,92
Ta 154,3,8704
C 78,12
Mvaa 124,96,80
E 15
Mvaa 124,100,84
Ta 152,3,10752
C 79,14
Mvva 200,98,128
U 8
U 9
Mvva 200,94,132
E 8
Mvva 204,94,140
U 10
U 11
Mvva 204,98,136
s_waitcnt vmcnt(10)
E 9
Mava 252,112,136
s_waitcnt lgkmcnt(0)
E 10
Mava 252,108,140
s_barrier
Ba 96,1,16384
C 92,8
Mava 248,108,132
E 11
Mava 248,112,128
Ba 100,1,16640
C 93,10
Yvaa 8,112,0,16
U 124
U 125
Mvaa 8,116,4
E 124
Mvaa 8,120,8
s_add_u32 m0, s41, 8192
U 126
U 127
Mvaa 8,124,12
buffer_load_dwordx4 v4, s[56:59], s43 offen lds
E 125
Yvaa 12,120,24,16
E 126
Mvaa 12,124,28
Ba 104,1,16896
C 106,124
Mvaa 12,112,16
E 127
Mvaa 12,116,20
Ba 108,1,17152
C 107,126
Mvva 208,42,144
U 8
U 9
Mvva 208,38,148
E 8
Mvva 212,38,156
U 10
U 11
Mvva 212,42,152
buffer_load_dwordx4 v4, s[56:59], s43 offen offset:1024 lds
E 9
Mvva 220,56,152
s_xor_b32 s43, s43, 0x6000
E 10
Mvva 220,52,156
Ta 128,2,16384
C 38,8
Mvva 216,52,148
E 11
Mvva 216,56,144
Ta 130,2,18432
C 39,10
Yvaa 8,112,32,20
U 12
U 13
Mvaa 8,116,36
E 12
Mvaa 8,120,40
s_add_u32 m0, s41, 40960
U 14
U 15
Mvaa 8,124,44
buffer_load_dwordx4 v28, s[60:63], s44 offen lds
E 13
Yvaa 124,120,56,20
E 14
Mvaa 124,124,60
Ta 132,2,20480
C 52,12
Mvaa 124,112,48
E 15
Mvaa 124,116,52
Ta 134,2,22528
C 53,14
Mvva 224,70,144
LPAD2%=:
s_cbranch_vccnz LRET%=
U 8
U 9
Mvva 224,66,148
E 8
Mvva 228,66,156
s_add_u32 m0, s41, 39936
U 10
U 11
Mvva 228,70,152
buffer_load_dwordx4 v28, s[60:63], s44 offen offset:2048 lds
E 9
Mvva 236,84,152
s_xor_b32 s44, s44, 0x1c080
E 10
Mvva 236,80,156
Ta 140,3,20480
C 66,8
Mvva 232,80,148
E 11
Mvva 232,84,144
Ta 142,3,22528
C 67,10
Yvaa 8,112,64,24
U 124
U 125
Mvaa 8,116,68
E 124
Mvaa 8,120,72
s_add_u32 m0, s42, 1024
U 126
U 127
Mvaa 8,124,76
buffer_load_dwordx4 v4, s[64:67], s45 offen lds
E 125
Yvaa 12,120,88,24
s_xor_b32 s45, s45, 0x3000
E 126
Mvaa 12,124,92
Ta 136,3,16384
C 80,124
Mvaa 12,112,80
E 127
Mvaa 12,116,84
Ta 138,3,18432
C 81,126
v_add_u32 v120, L0x36%=+2048-LPF%=, v122
Mvva 240,98,144
U 8
U 9
Mvva 240,94,148
buffer_load_dword v120, v120, s[72:75], 0 offen
E 8
Mvva 244,94,156
U 10
U 11
Mvva 244,98,152
E 9
)ASM"
R"ASM(
Mvva 252,112,152
s_waitcnt lgkmcnt(0)
E 10
Mvva 252,108,156
Ba 112,1,20480
C 94,8
Mvva 248,108,148
E 11
Mvva 248,112,144
Ba 116,1,20736
C 95,10
L0x36%=:
Yvaa 8,96,0,16
U 12
U 13
Mvaa 8,100,4
E 12
Mvaa 8,104,8
U 14
U 15
Mvaa 8,108,12
E 13
Yvaa 124,104,24,16
E 14
Mvaa 124,108,28
Ba 120,1,20992
C 108,12
Mvaa 124,96,16
E 15
Mvaa 124,100,20
Ba 124,1,21248
C 109,14
Mvva 128,32,128
U 8
U 9
Mvva 128,36,132
E 8
Mvva 132,36,140
U 10
U 11
Mvva 132,32,136
E 9
Mvva 140,46,136
E 10
Mvva 140,50,140
Ta 144,2,16896
C 44,8
Mvva 136,50,132
E 11
Mvva 136,46,128
Ta 146,2,18944
C 45,10
Yvaa 8,96,32,20
U 124
U 125
Mvaa 8,100,36
E 124
Mvaa 8,104,40
U 126
U 127
Mvaa 8,108,44
buffer_load_dword v117, v116, s[64:67], s50 offen
E 125
Yvaa 12,104,56,20
s_add_u32 s50, s50, 4096
E 126
Mvaa 12,108,60
Ta 148,2,20992
C 58,124
Mvaa 12,96,48
E 127
Mvaa 12,100,52
Ta 150,2,23040
C 59,126
Mvva 144,60,128
U 8
U 9
Mvva 144,64,132
E 8
Mvva 148,64,140
U 10
U 11
Mvva 148,60,136
E 9
Mava 196,74,136
E 10
Mava 196,78,140
Ta 156,3,20992
C 72,8
Mava 192,78,132
E 11
Mava 192,74,128
Ta 158,3,23040
C 73,10
Yvaa 8,96,64,24
U 12
U 13
Mvaa 8,100,68
E 12
Mvaa 8,104,72
U 14
U 15
Mvaa 8,108,76
E 13
Yvaa 124,104,88,24
E 14
Mvaa 124,108,92
Ta 152,3,16896
C 86,12
Mvaa 124,96,80
E 15
Mvaa 124,100,84
s_add_u32 m0, s42, 2048
Ta 154,3,18944
C 87,14
v_smfmac_f32_16x16x64_bf16 a[160:163], a[184:187], v[32:39], v7
buffer_load_dwordx4 v0, s[64:67], s45 offen lds
Mvva 152,88,128
s_xor_b32 s45, s45, 0x1000
U 8
U 9
Mvva 152,92,132
E 8
Mvva 156,92,140
U 10
U 11
Mvva 156,88,136
s_waitcnt vmcnt(13)
E 9
Mava 204,102,136
s_waitcnt lgkmcnt(0)
E 10
Mava 204,106,140
s_barrier
Ba 96,1,24576
C 100,8
Mava 200,106,132
E 11
Mava 200,102,128
Ba 100,1,24832
C 101,10
Yvaa 8,112,0,16
U 124
U 125
Mvaa 8,116,4
E 124
Mvaa 8,120,8
U 126
U 127
Mvaa 8,124,12
E 125
Yvaa 12,120,24,16
E 126
Mvaa 12,124,28
Ba 104,1,25088
C 114,124
Mvaa 12,112,16
E 127
Mvaa 12,116,20
s_add_u32 m0, s41, 16384
Ba 108,1,25344
C 115,126
v_smfmac_f32_16x16x64_bf16 a[164:167], a[184:187], v[46:53], v7
buffer_load_dwordx4 v4, s[56:59], s43 offen lds
Mvva 160,32,144
U 8
U 9
Mvva 160,36,148
E 8
Mvva 164,36,156
U 10
U 11
Mvva 164,32,152
buffer_load_dwordx4 v4, s[56:59], s43 offen offset:1024 lds
E 9
Mava 212,46,152
s_xor_b32 s43, s43, 0x2000
E 10
Mava 212,50,156
Ta 128,2,24576
C 42,8
Mava 208,50,148
E 11
Mava 208,46,144
Ta 130,2,26624
C 43,10
Yvaa 8,112,32,20
U 12
U 13
Mvaa 8,116,36
E 12
Mvaa 8,120,40
U 14
U 15
Mvaa 8,124,44
E 13
Yvaa 124,120,56,20
E 14
Mvaa 124,124,60
Ta 132,2,28672
C 56,12
Mvaa 124,112,48
E 15
Mvaa 124,116,52
s_add_u32 m0, s41, 49152
Ta 134,2,30720
C 57,14
v_smfmac_f32_16x16x64_bf16 a[168:171], a[184:187], v[60:67], v7
buffer_load_dwordx4 v28, s[60:63], s44 offen lds
Mvva 168,60,144
U 8
U 9
Mvva 168,64,148
E 8
Mvva 172,64,156
s_add_u32 m0, s41, 48128
U 10
U 11
Mvva 172,60,152
buffer_load_dwordx4 v28, s[60:63], s44 offen offset:2048 lds
E 9
Mava 220,74,152
s_xor_b32 s44, s44, 0x80
E 10
Mava 220,78,156
Ta 140,3,28672
C 70,8
Mava 216,78,148
E 11
Mava 216,74,144
Ta 142,3,30720
C 71,10
Yvaa 8,112,64,24
U 124
U 125
Mvaa 8,116,68
E 124
Mvaa 8,120,72
s_add_u32 m0, s42, 3072
U 126
U 127
Mvaa 8,124,76
buffer_load_dwordx4 v0, s[64:67], s45 offen lds
E 125
Yvaa 12,120,88,24
s_xor_b32 s45, s45, 0x7000
E 126
Mvaa 12,124,92
Ta 136,3,24576
C 84,124
Mvaa 12,112,80
E 127
Mvaa 12,116,84
Ta 138,3,26624
C 85,126
Mvva 176,88,144
U 8
U 9
Mvva 176,92,148
E 8
Mvva 180,92,156
U 10
U 11
Mvva 180,88,152
E 9
Mava 228,102,152
s_waitcnt lgkmcnt(0)
E 10
Mava 228,106,156
Ba 112,1,28672
C 98,8
Mava 224,106,148
E 11
Mava 224,102,144
Ba 116,1,28928
C 99,10
Yvaa 8,96,0,16
U 12
U 13
Mvaa 8,100,4
E 12
Mvaa 8,104,8
U 14
U 15
Mvaa 8,108,12
E 13
Yvaa 124,104,24,16
E 14
Mvaa 124,108,28
Ba 120,1,29184
C 112,12
Mvaa 124,96,16
E 15
Mvaa 124,100,20
Ba 124,1,29440
C 113,14
Mvva 184,32,128
U 8
U 9
Mvva 184,36,132
E 8
Mvva 188,36,140
U 10
U 11
Mvva 188,32,136
E 9
Mava 236,46,136
E 10
Mava 236,50,140
Ta 144,2,25088
C 40,8
Mava 232,50,132
E 11
Mava 232,46,128
Ta 146,2,27136
C 41,10
Yvaa 8,96,32,20
U 124
U 125
Mvaa 8,100,36
E 124
Mvaa 8,104,40
U 126
U 127
Mvaa 8,108,44
E 125
Yvaa 12,104,56,20
E 126
Mvaa 12,108,60
Ta 148,2,29184
C 54,124
Mvaa 12,96,48
E 127
Mvaa 12,100,52
Ta 150,2,31232
C 55,126
Mvva 192,60,128
U 8
U 9
Mvva 192,64,132
E 8
Mvva 196,64,140
U 10
U 11
Mvva 196,60,136
E 9
Mava 244,74,136
E 10
Mava 244,78,140
Ta 156,3,29184
C 68,8
Mava 240,78,132
E 11
Mava 240,74,128
Ta 158,3,31232
C 69,10
Yvaa 8,96,64,24
U 12
U 13
Mvaa 8,100,68
E 12
Mvaa 8,104,72
U 14
U 15
Mvaa 8,108,76
E 13
Yvaa 124,104,88,24
E 14
Mvaa 124,108,92
Ta 152,3,25088
C 82,12
Mvaa 124,96,80
E 15
Mvaa 124,100,84
s_add_u32 m0, s42, 4096
Ta 154,3,27136
C 83,14
v_smfmac_f32_16x16x64_bf16 a[172:175], a[184:187], v[74:81], v7
buffer_load_dwordx4 v4, s[64:67], s45 offen lds
Mvva 200,88,128
s_xor_b32 s45, s45, 0x1000
U 8
U 9
Mvva 200,92,132
E 8
Mvva 204,92,140
U 10
U 11
Mvva 204,88,136
s_waitcnt vmcnt(10)
E 9
Mava 252,102,136
s_waitcnt lgkmcnt(0)
E 10
Mava 252,106,140
s_barrier
Ba 96,1,0
C 96,8
Mava 248,106,132
E 11
)ASM"
R"ASM(
Mava 248,102,128
Ba 100,1,256
C 97,10
Yvaa 8,112,0,16
U 124
U 125
Mvaa 8,116,4
E 124
Mvaa 8,120,8
U 126
U 127
Mvaa 8,124,12
E 125
Yvaa 12,120,24,16
E 126
Mvaa 12,124,28
Ba 104,1,512
C 110,124
Mvaa 12,112,16
E 127
Mvaa 12,116,20
s_add_u32 m0, s41, 24576
Ba 108,1,768
C 111,126
v_smfmac_f32_16x16x64_bf16 a[176:179], a[184:187], v[88:95], v7
buffer_load_dwordx4 v4, s[56:59], s43 offen lds
Mvva 208,32,144
U 8
U 9
Mvva 208,36,148
E 8
Mvva 212,36,156
U 10
U 11
Mvva 212,32,152
buffer_load_dwordx4 v4, s[56:59], s43 offen offset:1024 lds
E 9
Mvva 220,46,152
s_xor_b32 s43, s43, 0xe000
E 10
Mvva 220,50,156
Ta 130,2,0
C 38,8
Mvva 216,50,148
E 11
Mvva 216,46,144
Ta 128,2,2048
C 39,10
Yvaa 8,112,32,20
U 12
U 13
Mvaa 8,116,36
E 12
Mvaa 8,120,40
U 14
U 15
Mvaa 8,124,44
E 13
Yvaa 124,120,56,20
E 14
Mvaa 124,124,60
Ta 134,2,4096
C 52,12
Mvaa 124,112,48
E 15
Mvaa 124,116,52
s_add_u32 m0, s41, 57344
Ta 132,2,6144
C 53,14
v_smfmac_f32_16x16x64_bf16 a[180:183], a[184:187], v[102:109], v7
buffer_load_dwordx4 v28, s[60:63], s44 offen lds
Mvva 224,60,144
U 8
U 9
Mvva 224,64,148
E 8
Mvva 228,64,156
s_add_u32 m0, s41, 56320
U 10
U 11
Mvva 228,60,152
buffer_load_dwordx4 v28, s[60:63], s44 offen offset:2048 lds
E 9
Mvva 236,74,152
s_xor_b32 s44, s44, 0x4080
E 10
Mvva 236,78,156
Ta 142,3,4096
C 66,8
Mvva 232,78,148
E 11
Mvva 232,74,144
Ta 140,3,6144
C 67,10
Yvaa 8,112,64,24
U 124
U 125
Mvaa 8,116,68
E 124
Mvaa 8,120,72
s_add_u32 m0, s42, 5120
U 126
U 127
Mvaa 8,124,76
buffer_load_dwordx4 v4, s[64:67], s45 offen lds
E 125
Yvaa 12,120,88,24
s_xor_b32 s45, s45, 0x3000
E 126
Mvaa 12,124,92
Ta 138,3,0
C 80,124
Mvaa 12,112,80
E 127
Mvaa 12,116,84
Ta 136,3,2048
C 81,126
v_add_u32 v120, L0x60%=-LPF%=, v122
Mvva 240,88,144
U 8
U 9
Mvva 240,92,148
buffer_load_dword v120, v120, s[72:75], 0 offen
E 8
Mvva 244,92,156
U 10
U 11
Mvva 244,88,152
E 9
Mvva 252,102,152
s_waitcnt lgkmcnt(0)
E 10
Mvva 252,106,156
Ba 112,1,4096
C 94,8
Mvva 248,106,148
E 11
Mvva 248,102,144
Ba 116,1,4352
C 95,10
L0x48%=:
Yvaa 8,96,0,16
U 12
U 13
Mvaa 8,100,4
E 12
Mvaa 8,104,8
U 14
U 15
Mvaa 8,108,12
E 13
Yvaa 124,104,24,16
E 14
Mvaa 124,108,28
Ba 120,1,4608
C 108,12
Mvaa 124,96,16
E 15
Mvaa 124,100,20
Ba 124,1,4864
C 109,14
Mvva 128,42,128
U 8
U 9
Mvva 128,38,132
E 8
Mvva 132,38,140
U 10
U 11
Mvva 132,42,136
E 9
Mvva 140,56,136
E 10
Mvva 140,52,140
LPAD3%=:
s_cbranch_vccnz LRET%=
Ta 146,2,512
C 32,8
Mvva 136,52,132
E 11
Mvva 136,56,128
Ta 144,2,2560
C 33,10
Yvaa 8,96,32,20
U 124
U 125
Mvaa 8,100,36
E 124
Mvaa 8,104,40
U 126
U 127
Mvaa 8,108,44
buffer_load_dword v117, v116, s[64:67], s50 offen
E 125
Yvaa 12,104,56,20
s_add_u32 s50, s50, 4096
E 126
Mvaa 12,108,60
Ta 150,2,4608
C 46,124
Mvaa 12,96,48
E 127
Mvaa 12,100,52
Ta 148,2,6656
C 47,126
Mvva 144,70,128
U 8
U 9
Mvva 144,66,132
E 8
Mvva 148,66,140
U 10
U 11
Mvva 148,70,136
E 9
Mava 196,84,136
E 10
Mava 196,80,140
Ta 158,3,4608
C 60,8
Mava 192,80,132
E 11
Mava 192,84,128
Ta 156,3,6656
C 61,10
Yvaa 8,96,64,24
U 12
U 13
Mvaa 8,100,68
E 12
Mvaa 8,104,72
U 14
U 15
Mvaa 8,108,76
E 13
Yvaa 124,104,88,24
E 14
Mvaa 124,108,92
Ta 154,3,512
C 74,12
Mvaa 124,96,80
E 15
Mvaa 124,100,84
s_add_u32 m0, s42, 6144
Ta 152,3,2560
C 75,14
v_smfmac_f32_16x16x64_bf16 a[160:163], a[184:187], v[38:45], v7
buffer_load_dwordx4 v0, s[64:67], s45 offen lds
Mvva 152,98,128
s_xor_b32 s45, s45, 0x1000
U 8
U 9
Mvva 152,94,132
E 8
Mvva 156,94,140
U 10
U 11
Mvva 156,98,136
E 9
Mava 204,112,136
s_waitcnt lgkmcnt(0)
E 10
Mava 204,108,140
Ba 96,1,8192
C 88,8
Mava 200,108,132
E 11
Mava 200,112,128
Ba 100,1,8448
C 89,10
Yvaa 8,112,0,16
U 124
U 125
Mvaa 8,116,4
E 124
Mvaa 8,120,8
U 126
U 127
Mvaa 8,124,12
E 125
Yvaa 12,120,24,16
E 126
Mvaa 12,124,28
Ba 104,1,8704
C 102,124
Mvaa 12,112,16
E 127
Mvaa 12,116,20
Ba 108,1,8960
C 103,126
v_smfmac_f32_16x16x64_bf16 a[164:167], a[184:187], v[52:59], v7
Mvva 160,42,144
U 8
U 9
Mvva 160,38,148
E 8
Mvva 164,38,156
U 10
U 11
Mvva 164,42,152
E 9
Mava 212,56,152
E 10
Mava 212,52,156
Ta 130,2,8192
C 34,8
Mava 208,52,148
E 11
Mava 208,56,144
Ta 128,2,10240
C 35,10
Yvaa 8,112,32,20
U 12
U 13
Mvaa 8,116,36
E 12
Mvaa 8,120,40
U 14
U 15
Mvaa 8,124,44
E 13
Yvaa 124,120,56,20
E 14
Mvaa 124,124,60
Ta 134,2,12288
C 48,12
Mvaa 124,112,48
E 15
Mvaa 124,116,52
Ta 132,2,14336
C 49,14
Mvva 168,70,144
U 8
U 9
Mvva 168,66,148
E 8
Mvva 172,66,156
U 10
U 11
Mvva 172,70,152
E 9
Mava 220,84,152
E 10
Mava 220,80,156
Ta 142,3,12288
C 62,8
Mava 216,80,148
E 11
Mava 216,84,144
Ta 140,3,14336
C 63,10
Yvaa 8,112,64,24
U 124
U 125
Mvaa 8,116,68
E 124
Mvaa 8,120,72
U 126
U 127
Mvaa 8,124,76
E 125
Yvaa 12,120,88,24
E 126
Mvaa 12,124,92
Ta 138,3,8192
C 76,124
Mvaa 12,112,80
E 127
Mvaa 12,116,84
s_add_u32 m0, s42, 7168
Ta 136,3,10240
C 77,126
v_smfmac_f32_16x16x64_bf16 a[168:171], a[184:187], v[66:73], v7
buffer_load_dwordx4 v0, s[64:67], s45 offen lds
Mvva 176,98,144
s_xor_b32 s45, s45, 0x7000
U 8
U 9
Mvva 176,94,148
s_add_u32 s45, s45, 0x8000
E 8
Mvva 180,94,156
U 10
U 11
Mvva 180,98,152
E 9
Mava 228,112,152
s_waitcnt lgkmcnt(0)
E 10
Mava 228,108,156
Ba 112,1,12288
C 90,8
Mava 224,108,148
E 11
Mava 224,112,144
Ba 116,1,12544
C 91,10
Yvaa 8,96,0,16
U 12
U 13
)ASM"
R"ASM(
Mvaa 8,100,4
E 12
Mvaa 8,104,8
U 14
U 15
Mvaa 8,108,12
E 13
Yvaa 124,104,24,16
E 14
Mvaa 124,108,28
Ba 120,1,12800
C 104,12
Mvaa 124,96,16
E 15
Mvaa 124,100,20
Ba 124,1,13056
C 105,14
Mvva 184,42,128
U 8
U 9
Mvva 184,38,132
E 8
Mvva 188,38,140
U 10
U 11
Mvva 188,42,136
E 9
Mava 236,56,136
E 10
Mava 236,52,140
Ta 146,2,8704
C 36,8
Mava 232,52,132
E 11
Mava 232,56,128
Ta 144,2,10752
C 37,10
Yvaa 8,96,32,20
U 124
U 125
Mvaa 8,100,36
E 124
Mvaa 8,104,40
U 126
U 127
Mvaa 8,108,44
E 125
Yvaa 12,104,56,20
E 126
Mvaa 12,108,60
Ta 150,2,12800
C 50,124
Mvaa 12,96,48
E 127
Mvaa 12,100,52
Ta 148,2,14848
C 51,126
Mvva 192,70,128
U 8
U 9
Mvva 192,66,132
E 8
Mvva 196,66,140
U 10
U 11
Mvva 196,70,136
E 9
Mava 244,84,136
E 10
Mava 244,80,140
Ta 158,3,12800
C 64,8
Mava 240,80,132
E 11
Mava 240,84,128
Ta 156,3,14848
C 65,10
Yvaa 8,96,64,24
U 12
U 13
Mvaa 8,100,68
E 12
Mvaa 8,104,72
U 14
U 15
Mvaa 8,108,76
E 13
Yvaa 124,104,88,24
E 14
Mvaa 124,108,92
Ta 154,3,8704
C 78,12
Mvaa 124,96,80
E 15
Mvaa 124,100,84
s_add_u32 m0, s42, 8192
Ta 152,3,10752
C 79,14
v_smfmac_f32_16x16x64_bf16 a[172:175], a[184:187], v[80:87], v7
buffer_load_dwordx4 v4, s[64:67], s45 offen lds
Mvva 200,98,128
s_xor_b32 s45, s45, 0x1000
U 8
U 9
Mvva 200,94,132
E 8
Mvva 204,94,140
U 10
U 11
Mvva 204,98,136
s_waitcnt vmcnt(6)
E 9
Mava 252,112,136
s_waitcnt lgkmcnt(0)
E 10
Mava 252,108,140
s_barrier
Ba 96,1,16384
C 92,8
Mava 248,108,132
E 11
Mava 248,112,128
Ba 100,1,16640
C 93,10
Yvaa 8,112,0,16
U 124
U 125
Mvaa 8,116,4
E 124
Mvaa 8,120,8
U 126
U 127
Mvaa 8,124,12
E 125
Yvaa 12,120,24,16
E 126
Mvaa 12,124,28
Ba 104,1,16896
C 106,124
Mvaa 12,112,16
E 127
Mvaa 12,116,20
s_add_u32 m0, s41, 0
Ba 108,1,17152
C 107,126
v_smfmac_f32_16x16x64_bf16 a[176:179], a[184:187], v[94:101], v7
buffer_load_dwordx4 v4, s[56:59], s43 offen lds
Mvva 208,42,144
U 8
U 9
Mvva 208,38,148
E 8
Mvva 212,38,156
U 10
U 11
Mvva 212,42,152
buffer_load_dwordx4 v4, s[56:59], s43 offen offset:1024 lds
E 9
Mvva 220,56,152
s_xor_b32 s43, s43, 0x2000
E 10
Mvva 220,52,156
Ta 128,2,16384
C 38,8
Mvva 216,52,148
E 11
Mvva 216,56,144
Ta 130,2,18432
C 39,10
Yvaa 8,112,32,20
U 12
U 13
Mvaa 8,116,36
E 12
Mvaa 8,120,40
U 14
U 15
Mvaa 8,124,44
buffer_load_dword v117, v116, s[64:67], s50 offen
E 13
Yvaa 124,120,56,20
s_add_u32 s50, s50, 4096
E 14
Mvaa 124,124,60
Ta 132,2,20480
C 52,12
Mvaa 124,112,48
E 15
Mvaa 124,116,52
s_add_u32 m0, s41, 32768
Ta 134,2,22528
C 53,14
v_smfmac_f32_16x16x64_bf16 a[180:183], a[184:187], v[108:115], v7
buffer_load_dwordx4 v28, s[60:63], s44 offen lds
Mvva 224,70,144
U 8
U 9
Mvva 224,66,148
E 8
Mvva 228,66,156
s_add_u32 m0, s41, 31744
U 10
U 11
Mvva 228,70,152
buffer_load_dwordx4 v28, s[60:63], s44 offen offset:2048 lds
E 9
Mvva 236,84,152
s_xor_b32 s44, s44, 0x80
E 10
Mvva 236,80,156
Ta 140,3,20480
C 66,8
Mvva 232,80,148
E 11
Mvva 232,84,144
Ta 142,3,22528
C 67,10
Yvaa 8,112,64,24
U 124
U 125
Mvaa 8,116,68
E 124
Mvaa 8,120,72
s_add_u32 m0, s42, 9216
U 126
U 127
Mvaa 8,124,76
buffer_load_dwordx4 v4, s[64:67], s45 offen lds
E 125
Yvaa 12,120,88,24
s_xor_b32 s45, s45, 0x3000
E 126
Mvaa 12,124,92
Ta 136,3,16384
C 80,124
Mvaa 12,112,80
E 127
Mvaa 12,116,84
Ta 138,3,18432
C 81,126
v_add_u32 v120, L0x84%=-LPF%=, v122
Mvva 240,98,144
U 8
U 9
Mvva 240,94,148
buffer_load_dword v120, v120, s[72:75], 0 offen
E 8
Mvva 244,94,156
U 10
U 11
Mvva 244,98,152
E 9
Mvva 252,112,152
s_waitcnt lgkmcnt(0)
E 10
Mvva 252,108,156
Ba 112,1,20480
C 94,8
Mvva 248,108,148
E 11
Mvva 248,112,144
Ba 116,1,20736
C 95,10
L0x60%=:
Yvaa 8,96,0,16
U 12
U 13
Mvaa 8,100,4
E 12
Mvaa 8,104,8
U 14
U 15
Mvaa 8,108,12
E 13
Yvaa 124,104,24,16
E 14
Mvaa 124,108,28
Ba 120,1,20992
C 108,12
Mvaa 124,96,16
E 15
Mvaa 124,100,20
s_add_u32 m0, s41, 8192
Ba 124,1,21248
C 109,14
v_smfmac_f32_16x16x64_bf16 a[160:163], a[184:187], v[32:39], v7
buffer_load_dwordx4 v4, s[56:59], s43 offen lds
Mvva 128,32,128
U 8
U 9
Mvva 128,36,132
E 8
Mvva 132,36,140
U 10
U 11
Mvva 132,32,136
buffer_load_dwordx4 v4, s[56:59], s43 offen offset:1024 lds
E 9
Mvva 140,46,136
s_xor_b32 s43, s43, 0x6000
E 10
Mvva 140,50,140
Ta 144,2,16896
C 44,8
Mvva 136,50,132
E 11
Mvva 136,46,128
Ta 146,2,18944
C 45,10
Yvaa 8,96,32,20
U 124
U 125
Mvaa 8,100,36
E 124
Mvaa 8,104,40
U 126
U 127
Mvaa 8,108,44
E 125
Yvaa 12,104,56,20
E 126
Mvaa 12,108,60
Ta 148,2,20992
C 58,124
Mvaa 12,96,48
E 127
Mvaa 12,100,52
s_add_u32 m0, s41, 40960
Ta 150,2,23040
C 59,126
v_smfmac_f32_16x16x64_bf16 a[164:167], a[184:187], v[46:53], v7
buffer_load_dwordx4 v28, s[60:63], s44 offen lds
Mvva 144,60,128
U 8
U 9
Mvva 144,64,132
E 8
Mvva 148,64,140
s_add_u32 m0, s41, 39936
U 10
U 11
Mvva 148,60,136
buffer_load_dwordx4 v28, s[60:63], s44 offen offset:2048 lds
E 9
Mava 196,74,136
s_xor_b32 s44, s44, 0xc080
E 10
Mava 196,78,140
Ta 156,3,20992
C 72,8
Mava 192,78,132
E 11
Mava 192,74,128
Ta 158,3,23040
C 73,10
Yvaa 8,96,64,24
U 12
U 13
Mvaa 8,100,68
E 12
Mvaa 8,104,72
s_add_u32 m0, s42, 10240
U 14
U 15
Mvaa 8,108,76
buffer_load_dwordx4 v0, s[64:67], s45 offen lds
E 13
Yvaa 124,104,88,24
s_xor_b32 s45, s45, 0x1000
E 14
Mvaa 124,108,92
Ta 152,3,16896
C 86,12
Mvaa 124,96,80
E 15
Mvaa 124,100,84
Ta 154,3,18944
C 87,14
Mvva 152,88,128
U 8
U 9
Mvva 152,92,132
E 8
Mvva 156,92,140
U 10
U 11
Mvva 156,88,136
E 9
Mava 204,102,136
s_waitcnt lgkmcnt(0)
E 10
Mava 204,106,140
Ba 96,1,24576
C 100,8
Mava 200,106,132
E 11
Mava 200,102,128
Ba 100,1,24832
C 101,10
Yvaa 8,112,0,16
)ASM"
R"ASM(
U 124
U 125
Mvaa 8,116,4
E 124
Mvaa 8,120,8
U 126
U 127
Mvaa 8,124,12
LPAD4%=:
s_cbranch_vccnz LRET%=
E 125
Yvaa 12,120,24,16
E 126
Mvaa 12,124,28
Ba 104,1,25088
C 114,124
Mvaa 12,112,16
E 127
Mvaa 12,116,20
Ba 108,1,25344
C 115,126
Mvva 160,32,144
U 8
U 9
Mvva 160,36,148
E 8
Mvva 164,36,156
U 10
U 11
Mvva 164,32,152
E 9
Mava 212,46,152
E 10
Mava 212,50,156
Ta 128,2,24576
C 42,8
Mava 208,50,148
E 11
Mava 208,46,144
Ta 130,2,26624
C 43,10
Yvaa 8,112,32,20
U 12
U 13
Mvaa 8,116,36
E 12
Mvaa 8,120,40
U 14
U 15
Mvaa 8,124,44
E 13
Yvaa 124,120,56,20
E 14
Mvaa 124,124,60
Ta 132,2,28672
C 56,12
Mvaa 124,112,48
E 15
Mvaa 124,116,52
Ta 134,2,30720
C 57,14
Mvva 168,60,144
U 8
U 9
Mvva 168,64,148
E 8
Mvva 172,64,156
U 10
U 11
Mvva 172,60,152
E 9
Mava 220,74,152
E 10
Mava 220,78,156
Ta 140,3,28672
C 70,8
Mava 216,78,148
E 11
Mava 216,74,144
Ta 142,3,30720
C 71,10
Yvaa 8,112,64,24
U 124
U 125
Mvaa 8,116,68
E 124
Mvaa 8,120,72
U 126
U 127
Mvaa 8,124,76
E 125
Yvaa 12,120,88,24
E 126
Mvaa 12,124,92
Ta 136,3,24576
C 84,124
Mvaa 12,112,80
E 127
Mvaa 12,116,84
s_add_u32 m0, s42, 11264
Ta 138,3,26624
C 85,126
v_smfmac_f32_16x16x64_bf16 a[168:171], a[184:187], v[60:67], v7
buffer_load_dwordx4 v0, s[64:67], s45 offen lds
Mvva 176,88,144
s_xor_b32 s45, s45, 0x7000
U 8
U 9
Mvva 176,92,148
E 8
Mvva 180,92,156
U 10
U 11
Mvva 180,88,152
E 9
Mava 228,102,152
s_waitcnt lgkmcnt(0)
E 10
Mava 228,106,156
Ba 112,1,28672
C 98,8
Mava 224,106,148
E 11
Mava 224,102,144
Ba 116,1,28928
C 99,10
Yvaa 8,96,0,16
U 12
U 13
Mvaa 8,100,4
E 12
Mvaa 8,104,8
U 14
U 15
Mvaa 8,108,12
E 13
Yvaa 124,104,24,16
E 14
Mvaa 124,108,28
Ba 120,1,29184
C 112,12
Mvaa 124,96,16
E 15
Mvaa 124,100,20
Ba 124,1,29440
C 113,14
Mvva 184,32,128
U 8
U 9
Mvva 184,36,132
E 8
Mvva 188,36,140
U 10
U 11
Mvva 188,32,136
E 9
Mava 236,46,136
E 10
Mava 236,50,140
Ta 144,2,25088
C 40,8
Mava 232,50,132
E 11
Mava 232,46,128
Ta 146,2,27136
C 41,10
Yvaa 8,96,32,20
U 124
U 125
Mvaa 8,100,36
E 124
Mvaa 8,104,40
U 126
U 127
Mvaa 8,108,44
E 125
Yvaa 12,104,56,20
E 126
Mvaa 12,108,60
Ta 148,2,29184
C 54,124
Mvaa 12,96,48
E 127
Mvaa 12,100,52
Ta 150,2,31232
C 55,126
Mvva 192,60,128
U 8
U 9
Mvva 192,64,132
E 8
Mvva 196,64,140
U 10
U 11
Mvva 196,60,136
E 9
Mava 244,74,136
E 10
Mava 244,78,140
Ta 156,3,29184
C 68,8
Mava 240,78,132
E 11
Mava 240,74,128
Ta 158,3,31232
C 69,10
Yvaa 8,96,64,24
U 12
U 13
Mvaa 8,100,68
E 12
Mvaa 8,104,72
U 14
U 15
Mvaa 8,108,76
E 13
Yvaa 124,104,88,24
E 14
Mvaa 124,108,92
Ta 152,3,25088
C 82,12
Mvaa 124,96,80
E 15
Mvaa 124,100,84
s_add_u32 m0, s42, 12288
Ta 154,3,27136
C 83,14
v_smfmac_f32_16x16x64_bf16 a[172:175], a[184:187], v[74:81], v7
buffer_load_dwordx4 v4, s[64:67], s45 offen lds
Mvva 200,88,128
s_xor_b32 s45, s45, 0x1000
U 8
U 9
Mvva 200,92,132
E 8
Mvva 204,92,140
U 10
U 11
Mvva 204,88,136
s_waitcnt vmcnt(9)
E 9
Mava 252,102,136
s_waitcnt lgkmcnt(0)
E 10
Mava 252,106,140
s_barrier
Ba 96,1,0
C 96,8
Mava 248,106,132
E 11
Mava 248,102,128
Ba 100,1,256
C 97,10
Yvaa 8,112,0,16
U 124
U 125
Mvaa 8,116,4
E 124
Mvaa 8,120,8
U 126
U 127
Mvaa 8,124,12
E 125
Yvaa 12,120,24,16
E 126
Mvaa 12,124,28
Ba 104,1,512
C 110,124
Mvaa 12,112,16
E 127
Mvaa 12,116,20
s_add_u32 m0, s41, 16384
Ba 108,1,768
C 111,126
v_smfmac_f32_16x16x64_bf16 a[176:179], a[184:187], v[88:95], v7
buffer_load_dwordx4 v4, s[56:59], s43 offen lds
Mvva 208,32,144
U 8
U 9
Mvva 208,36,148
E 8
Mvva 212,36,156
U 10
U 11
Mvva 212,32,152
buffer_load_dwordx4 v4, s[56:59], s43 offen offset:1024 lds
E 9
Mvva 220,46,152
s_xor_b32 s43, s43, 0x2000
E 10
Mvva 220,50,156
Ta 130,2,0
C 38,8
Mvva 216,50,148
E 11
Mvva 216,46,144
Ta 128,2,2048
C 39,10
Yvaa 8,112,32,20
U 12
U 13
Mvaa 8,116,36
E 12
Mvaa 8,120,40
U 14
U 15
Mvaa 8,124,44
buffer_load_dword v117, v116, s[64:67], s50 offen
E 13
Yvaa 124,120,56,20
s_add_u32 s50, s50, 4096
E 14
Mvaa 124,124,60
Ta 134,2,4096
C 52,12
Mvaa 124,112,48
E 15
Mvaa 124,116,52
s_add_u32 m0, s41, 49152
Ta 132,2,6144
C 53,14
v_smfmac_f32_16x16x64_bf16 a[180:183], a[184:187], v[102:109], v7
buffer_load_dwordx4 v28, s[60:63], s44 offen lds
Mvva 224,60,144
U 8
U 9
Mvva 224,64,148
E 8
Mvva 228,64,156
s_add_u32 m0, s41, 48128
U 10
U 11
Mvva 228,60,152
buffer_load_dwordx4 v28, s[60:63], s44 offen offset:2048 lds
E 9
Mvva 236,74,152
s_xor_b32 s44, s44, 0x80
E 10
Mvva 236,78,156
Ta 142,3,4096
C 66,8
Mvva 232,78,148
E 11
Mvva 232,74,144
Ta 140,3,6144
C 67,10
Yvaa 8,112,64,24
U 124
U 125
Mvaa 8,116,68
E 124
Mvaa 8,120,72
s_add_u32 m0, s42, 13312
U 126
U 127
Mvaa 8,124,76
buffer_load_dwordx4 v4, s[64:67], s45 offen lds
E 125
Yvaa 12,120,88,24
s_xor_b32 s45, s45, 0x3000
E 126
Mvaa 12,124,92
Ta 138,3,0
C 80,124
Mvaa 12,112,80
E 127
Mvaa 12,116,84
Ta 136,3,2048
C 81,126
v_add_u32 v120, L2x0%=-LPF%=, v122
Mvva 240,88,144
U 8
U 9
Mvva 240,92,148
buffer_load_dword v120, v120, s[72:75], 0 offen
E 8
Mvva 244,92,156
U 10
U 11
Mvva 244,88,152
E 9
Mvva 252,102,152
s_waitcnt lgkmcnt(0)
E 10
Mvva 252,106,156
Ba 112,1,4096
C 94,8
Mvva 248,106,148
E 11
Mvva 248,102,144
Ba 116,1,4352
C 95,10
L0x72%=:
Yvaa 8,96,0,16
U 12
U 13
Mvaa 8,100,4
E 12
Mvaa 8,104,8
U 14
)ASM"
R"ASM(
U 15
Mvaa 8,108,12
E 13
Yvaa 124,104,24,16
E 14
Mvaa 124,108,28
Ba 120,1,4608
C 108,12
Mvaa 124,96,16
E 15
Mvaa 124,100,20
s_add_u32 m0, s41, 24576
Ba 124,1,4864
C 109,14
v_smfmac_f32_16x16x64_bf16 a[160:163], a[184:187], v[38:45], v7
buffer_load_dwordx4 v4, s[56:59], s43 offen lds
Mvva 128,42,128
U 8
U 9
Mvva 128,38,132
E 8
Mvva 132,38,140
U 10
U 11
Mvva 132,42,136
buffer_load_dwordx4 v4, s[56:59], s43 offen offset:1024 lds
E 9
Mvva 140,56,136
s_xor_b32 s43, s43, 0x1e000
E 10
Mvva 140,52,140
s_add_u32 s43, s43, 0x200000
Ta 146,2,512
C 32,8
Mvva 136,52,132
E 11
Mvva 136,56,128
Ta 144,2,2560
C 33,10
Yvaa 8,96,32,20
U 124
U 125
Mvaa 8,100,36
E 124
Mvaa 8,104,40
U 126
U 127
Mvaa 8,108,44
E 125
Yvaa 12,104,56,20
E 126
Mvaa 12,108,60
Ta 150,2,4608
C 46,124
Mvaa 12,96,48
E 127
Mvaa 12,100,52
s_add_u32 m0, s41, 57344
Ta 148,2,6656
C 47,126
v_smfmac_f32_16x16x64_bf16 a[164:167], a[184:187], v[52:59], v7
buffer_load_dwordx4 v28, s[60:63], s44 offen lds
Mvva 144,70,128
U 8
U 9
Mvva 144,66,132
E 8
Mvva 148,66,140
s_add_u32 m0, s41, 56320
U 10
U 11
Mvva 148,70,136
buffer_load_dwordx4 v28, s[60:63], s44 offen offset:2048 lds
E 9
Mava 196,84,136
s_xor_b32 s44, s44, 0x4080
E 10
Mava 196,80,140
Ta 158,3,4608
C 60,8
Mava 192,80,132
E 11
Mava 192,84,128
Ta 156,3,6656
C 61,10
Yvaa 8,96,64,24
U 12
U 13
Mvaa 8,100,68
E 12
Mvaa 8,104,72
s_add_u32 m0, s42, 14336
U 14
U 15
Mvaa 8,108,76
buffer_load_dwordx4 v0, s[64:67], s45 offen lds
E 13
Yvaa 124,104,88,24
s_xor_b32 s45, s45, 0x1000
E 14
Mvaa 124,108,92
Ta 154,3,512
C 74,12
Mvaa 124,96,80
E 15
Mvaa 124,100,84
Ta 152,3,2560
C 75,14
Mvva 152,98,128
U 8
U 9
Mvva 152,94,132
E 8
Mvva 156,94,140
U 10
U 11
Mvva 156,98,136
s_waitcnt vmcnt(15)
E 9
Mava 204,112,136
s_waitcnt lgkmcnt(0)
E 10
Mava 204,108,140
s_barrier
Ba 96,1,8192
C 88,8
Mava 200,108,132
E 11
Mava 200,112,128
Ba 100,1,8448
C 89,10
Yvaa 8,112,0,16
U 124
U 125
Mvaa 8,116,4
E 124
Mvaa 8,120,8
U 126
U 127
Mvaa 8,124,12
E 125
Yvaa 12,120,24,16
E 126
Mvaa 12,124,28
Ba 104,1,8704
C 102,124
Mvaa 12,112,16
E 127
Mvaa 12,116,20
s_add_u32 m0, s41, 0
Ba 108,1,8960
C 103,126
v_smfmac_f32_16x16x64_bf16 a[168:171], a[184:187], v[66:73], v7
buffer_load_dwordx4 v4, s[56:59], s43 offen lds
Mvva 160,42,144
U 8
U 9
Mvva 160,38,148
E 8
Mvva 164,38,156
U 10
U 11
Mvva 164,42,152
buffer_load_dwordx4 v4, s[56:59], s43 offen offset:1024 lds
E 9
Mava 212,56,152
s_xor_b32 s43, s43, 0x2000
E 10
Mava 212,52,156
Ta 130,2,8192
C 34,8
Mava 208,52,148
E 11
Mava 208,56,144
Ta 128,2,10240
C 35,10
Yvaa 8,112,32,20
U 12
U 13
Mvaa 8,116,36
E 12
Mvaa 8,120,40
U 14
U 15
Mvaa 8,124,44
E 13
Yvaa 124,120,56,20
E 14
Mvaa 124,124,60
Ta 134,2,12288
C 48,12
Mvaa 124,112,48
E 15
Mvaa 124,116,52
s_add_u32 m0, s41, 32768
Ta 132,2,14336
C 49,14
v_smfmac_f32_16x16x64_bf16 a[172:175], a[184:187], v[80:87], v7
buffer_load_dwordx4 v28, s[60:63], s44 offen lds
Mvva 168,70,144
U 8
U 9
Mvva 168,66,148
E 8
Mvva 172,66,156
s_add_u32 m0, s41, 31744
U 10
U 11
Mvva 172,70,152
buffer_load_dwordx4 v28, s[60:63], s44 offen offset:2048 lds
E 9
Mava 220,84,152
s_xor_b32 s44, s44, 0x80
E 10
Mava 220,80,156
Ta 142,3,12288
C 62,8
Mava 216,80,148
E 11
Mava 216,84,144
Ta 140,3,14336
C 63,10
Yvaa 8,112,64,24
U 124
U 125
Mvaa 8,116,68
E 124
Mvaa 8,120,72
s_add_u32 m0, s42, 15360
U 126
U 127
Mvaa 8,124,76
buffer_load_dwordx4 v0, s[64:67], s45 offen lds
E 125
Yvaa 12,120,88,24
s_xor_b32 s45, s45, 0x7000
E 126
Mvaa 12,124,92
s_add_u32 s45, s45, 0x8000
Ta 138,3,8192
C 76,124
Mvaa 12,112,80
LPAD5%=:
s_cbranch_vccnz LRET%=
E 127
Mvaa 12,116,84
Ta 136,3,10240
C 77,126
Mvva 176,98,144
U 8
U 9
Mvva 176,94,148
E 8
Mvva 180,94,156
U 10
U 11
Mvva 180,98,152
E 9
Mava 228,112,152
s_waitcnt lgkmcnt(0)
E 10
Mava 228,108,156
Ba 112,1,12288
C 90,8
Mava 224,108,148
E 11
Mava 224,112,144
Ba 116,1,12544
C 91,10
Yvaa 8,96,0,16
U 12
U 13
Mvaa 8,100,4
E 12
Mvaa 8,104,8
U 14
U 15
Mvaa 8,108,12
E 13
Yvaa 124,104,24,16
E 14
Mvaa 124,108,28
Ba 120,1,12800
C 104,12
Mvaa 124,96,16
E 15
Mvaa 124,100,20
Ba 124,1,13056
C 105,14
Mvva 184,42,128
U 8
U 9
Mvva 184,38,132
E 8
Mvva 188,38,140
U 10
U 11
Mvva 188,42,136
E 9
Mava 236,56,136
E 10
Mava 236,52,140
Ta 146,2,8704
C 36,8
Mava 232,52,132
E 11
Mava 232,56,128
Ta 144,2,10752
C 37,10
Yvaa 8,96,32,20
U 124
U 125
Mvaa 8,100,36
E 124
Mvaa 8,104,40
U 126
U 127
Mvaa 8,108,44
buffer_load_dword v117, v116, s[64:67], s50 offen
E 125
Yvaa 12,104,56,20
s_add_u32 s50, s50, 3125248
E 126
Mvaa 12,108,60
Ta 150,2,12800
C 50,124
Mvaa 12,96,48
E 127
Mvaa 12,100,52
Ta 148,2,14848
C 51,126
Mvva 192,70,128
U 8
U 9
Mvva 192,66,132
E 8
Mvva 196,66,140
U 10
U 11
Mvva 196,70,136
E 9
Mava 244,84,136
E 10
Mava 244,80,140
Ta 158,3,12800
C 64,8
Mava 240,80,132
E 11
Mava 240,84,128
Ta 156,3,14848
C 65,10
Yvaa 8,96,64,24
U 12
U 13
Mvaa 8,100,68
E 12
Mvaa 8,104,72
U 14
U 15
Mvaa 8,108,76
E 13
Yvaa 124,104,88,24
E 14
Mvaa 124,108,92
Ta 154,3,8704
C 78,12
Mvaa 124,96,80
E 15
Mvaa 124,100,84
s_add_u32 m0, s42, 16384
Ta 152,3,10752
C 79,14
v_smfmac_f32_16x16x64_bf16 a[176:179], a[184:187], v[94:101], v7
buffer_load_dwordx4 v4, s[64:67], s45 offen lds
Mvva 200,98,128
s_xor_b32 s45, s45, 0x1000
U 8
U 9
Mvva 200,94,132
E 8
Mvva 204,94,140
U 10
U 11
Mvva 204,98,136
s_waitcnt vmcnt(14)
E 9
Mava 252,112,136
s_waitcnt lgkmcnt(0)
E 10
Mava 252,108,140
s_barrier
Ba 96,1,16384
C 92,8
Mava 248,108,132
E 11
Mava 248,112,128
Ba 100,1,16640
C 93,10
Yvaa 8,112,0,16
U 124
)ASM"
R"ASM(
U 125
Mvaa 8,116,4
E 124
Mvaa 8,120,8
U 126
U 127
Mvaa 8,124,12
E 125
Yvaa 12,120,24,16
E 126
Mvaa 12,124,28
Ba 104,1,16896
C 106,124
Mvaa 12,112,16
E 127
Mvaa 12,116,20
s_add_u32 m0, s41, 8192
Ba 108,1,17152
C 107,126
v_smfmac_f32_16x16x64_bf16 a[180:183], a[184:187], v[108:115], v7
buffer_load_dwordx4 v4, s[56:59], s43 offen lds
Mvva 208,42,144
U 8
U 9
Mvva 208,38,148
E 8
Mvva 212,38,156
U 10
U 11
Mvva 212,42,152
buffer_load_dwordx4 v4, s[56:59], s43 offen offset:1024 lds
E 9
Mvva 220,56,152
s_xor_b32 s43, s43, 0x6000
E 10
Mvva 220,52,156
Ta 128,2,16384
C 38,8
Mvva 216,52,148
E 11
Mvva 216,56,144
Ta 130,2,18432
C 39,10
Yvaa 8,112,32,20
U 12
U 13
Mvaa 8,116,36
E 12
Mvaa 8,120,40
s_add_u32 m0, s41, 40960
U 14
U 15
Mvaa 8,124,44
buffer_load_dwordx4 v28, s[60:63], s44 offen lds
E 13
Yvaa 124,120,56,20
E 14
Mvaa 124,124,60
Ta 132,2,20480
C 52,12
Mvaa 124,112,48
E 15
Mvaa 124,116,52
Ta 134,2,22528
C 53,14
Mvva 224,70,144
U 8
U 9
Mvva 224,66,148
E 8
Mvva 228,66,156
s_add_u32 m0, s41, 39936
U 10
U 11
Mvva 228,70,152
buffer_load_dwordx4 v28, s[60:63], s44 offen offset:2048 lds
E 9
Mvva 236,84,152
s_xor_b32 s44, s44, 0x1c080
E 10
Mvva 236,80,156
s_add_u32 s44, s44, 0x200000
Ta 140,3,20480
C 66,8
Mvva 232,80,148
E 11
Mvva 232,84,144
Ta 142,3,22528
C 67,10
Yvaa 8,112,64,24
U 124
U 125
Mvaa 8,116,68
E 124
Mvaa 8,120,72
s_add_u32 m0, s42, 17408
U 126
U 127
Mvaa 8,124,76
buffer_load_dwordx4 v4, s[64:67], s45 offen lds
E 125
Yvaa 12,120,88,24
s_xor_b32 s45, s45, 0x3000
E 126
Mvaa 12,124,92
Ta 136,3,16384
C 80,124
Mvaa 12,112,80
E 127
Mvaa 12,116,84
Ta 138,3,18432
C 81,126
v_add_u32 v120, L2x0%=+8192-LPF%=, v122
Mvva 240,98,144
U 8
U 9
Mvva 240,94,148
buffer_load_dword v120, v120, s[72:75], 0 offen
E 8
Mvva 244,94,156
U 10
U 11
Mvva 244,98,152
E 9
Mvva 252,112,152
s_waitcnt lgkmcnt(0)
E 10
Mvva 252,108,156
Ba 112,1,20480
C 94,8
Mvva 248,108,148
E 11
Mvva 248,112,144
Ba 116,1,20736
C 95,10
L0x84%=:
Yvaa 8,96,0,16
U 12
U 13
Mvaa 8,100,4
E 12
Mvaa 8,104,8
U 14
U 15
Mvaa 8,108,12
E 13
Yvaa 124,104,24,16
E 14
Mvaa 124,108,28
Ba 120,1,20992
C 108,12
Mvaa 124,96,16
E 15
Mvaa 124,100,20
Ba 124,1,21248
C 109,14
Mvva 128,32,128
U 8
U 9
Mvva 128,36,132
E 8
Mvva 132,36,140
U 10
U 11
Mvva 132,32,136
E 9
Mvva 140,46,136
E 10
Mvva 140,50,140
Ta 144,2,16896
C 44,8
Mvva 136,50,132
E 11
Mvva 136,46,128
Ta 146,2,18944
C 45,10
Yvaa 8,96,32,20
U 124
U 125
Mvaa 8,100,36
E 124
Mvaa 8,104,40
U 126
U 127
Mvaa 8,108,44
E 125
Yvaa 12,104,56,20
E 126
Mvaa 12,108,60
Ta 148,2,20992
C 58,124
Mvaa 12,96,48
E 127
Mvaa 12,100,52
Ta 150,2,23040
C 59,126
Mvva 144,60,128
U 8
U 9
Mvva 144,64,132
E 8
Mvva 148,64,140
U 10
U 11
Mvva 148,60,136
E 9
Mava 196,74,136
E 10
Mava 196,78,140
Ta 156,3,20992
C 72,8
Mava 192,78,132
E 11
Mava 192,74,128
Ta 158,3,23040
C 73,10
Yvaa 8,96,64,24
U 12
U 13
Mvaa 8,100,68
E 12
Mvaa 8,104,72
U 14
U 15
Mvaa 8,108,76
E 13
Yvaa 124,104,88,24
E 14
Mvaa 124,108,92
Ta 152,3,16896
C 86,12
Mvaa 124,96,80
E 15
Mvaa 124,100,84
s_add_u32 m0, s42, 18432
Ta 154,3,18944
C 87,14
v_smfmac_f32_16x16x64_bf16 a[160:163], a[184:187], v[32:39], v7
buffer_load_dwordx4 v0, s[64:67], s45 offen lds
Mvva 152,88,128
s_xor_b32 s45, s45, 0x1000
U 8
U 9
Mvva 152,92,132
E 8
Mvva 156,92,140
U 10
U 11
Mvva 156,88,136
s_waitcnt vmcnt(15)
E 9
Mava 204,102,136
s_waitcnt lgkmcnt(0)
E 10
Mava 204,106,140
s_barrier
Ba 96,1,24576
C 100,8
Mava 200,106,132
E 11
Mava 200,102,128
Ba 100,1,24832
C 101,10
Yvaa 8,112,0,16
U 124
U 125
Mvaa 8,116,4
E 124
Mvaa 8,120,8
U 126
U 127
Mvaa 8,124,12
E 125
Yvaa 12,120,24,16
E 126
Mvaa 12,124,28
Ba 104,1,25088
C 114,124
Mvaa 12,112,16
E 127
Mvaa 12,116,20
s_add_u32 m0, s41, 16384
Ba 108,1,25344
C 115,126
v_smfmac_f32_16x16x64_bf16 a[164:167], a[184:187], v[46:53], v7
buffer_load_dwordx4 v4, s[56:59], s43 offen lds
Mvva 160,32,144
U 8
U 9
Mvva 160,36,148
E 8
Mvva 164,36,156
U 10
U 11
Mvva 164,32,152
buffer_load_dwordx4 v4, s[56:59], s43 offen offset:1024 lds
E 9
Mava 212,46,152
s_xor_b32 s43, s43, 0x2000
E 10
Mava 212,50,156
Ta 128,2,24576
C 42,8
Mava 208,50,148
E 11
Mava 208,46,144
Ta 130,2,26624
C 43,10
Yvaa 8,112,32,20
U 12
U 13
Mvaa 8,116,36
E 12
Mvaa 8,120,40
U 14
U 15
Mvaa 8,124,44
E 13
Yvaa 124,120,56,20
E 14
Mvaa 124,124,60
Ta 132,2,28672
C 56,12
Mvaa 124,112,48
E 15
Mvaa 124,116,52
s_add_u32 m0, s41, 49152
Ta 134,2,30720
C 57,14
v_smfmac_f32_16x16x64_bf16 a[168:171], a[184:187], v[60:67], v7
buffer_load_dwordx4 v28, s[60:63], s44 offen lds
Mvva 168,60,144
U 8
U 9
Mvva 168,64,148
E 8
Mvva 172,64,156
s_add_u32 m0, s41, 48128
U 10
U 11
Mvva 172,60,152
buffer_load_dwordx4 v28, s[60:63], s44 offen offset:2048 lds
E 9
Mava 220,74,152
s_xor_b32 s44, s44, 0x80
E 10
Mava 220,78,156
Ta 140,3,28672
C 70,8
Mava 216,78,148
E 11
Mava 216,74,144
Ta 142,3,30720
C 71,10
Yvaa 8,112,64,24
U 124
U 125
Mvaa 8,116,68
E 124
Mvaa 8,120,72
s_add_u32 m0, s42, 19456
U 126
U 127
Mvaa 8,124,76
buffer_load_dwordx4 v0, s[64:67], s45 offen lds
E 125
Yvaa 12,120,88,24
s_xor_b32 s45, s45, 0x7000
E 126
Mvaa 12,124,92
Ta 136,3,24576
C 84,124
Mvaa 12,112,80
E 127
Mvaa 12,116,84
Ta 138,3,26624
C 85,126
Mvva 176,88,144
U 8
U 9
Mvva 176,92,148
E 8
Mvva 180,92,156
U 10
U 11
Mvva 180,88,152
E 9
Mava 228,102,152
s_waitcnt lgkmcnt(0)
E 10
Mava 228,106,156
Ba 112,1,28672
C 98,8
Mava 224,106,148
E 11
Mava 224,102,144
Ba 116,1,28928
C 99,10
L0x90%=:
Yvaa 8,96,0,16
)ASM"
R"ASM(
U 12
U 13
Mvaa 8,100,4
E 12
Mvaa 8,104,8
U 14
U 15
Mvaa 8,108,12
E 13
Yvaa 124,104,24,16
E 14
Mvaa 124,108,28
Ba 120,1,29184
C 112,12
Mvaa 124,96,16
E 15
Mvaa 124,100,20
Ba 124,1,29440
C 113,14
Mvva 184,32,128
U 8
U 9
Mvva 184,36,132
E 8
Mvva 188,36,140
U 10
U 11
Mvva 188,32,136
E 9
Mava 236,46,136
E 10
Mava 236,50,140
Ta 144,2,25088
C 40,8
Mava 232,50,132
E 11
Mava 232,46,128
Ta 146,2,27136
C 41,10
Yvaa 8,96,32,20
U 124
U 125
Mvaa 8,100,36
E 124
Mvaa 8,104,40
U 126
U 127
Mvaa 8,108,44
E 125
Yvaa 12,104,56,20
E 126
Mvaa 12,108,60
Ta 148,2,29184
C 54,124
Mvaa 12,96,48
E 127
Mvaa 12,100,52
Ta 150,2,31232
C 55,126
Mvva 192,60,128
U 8
U 9
Mvva 192,64,132
E 8
Mvva 196,64,140
U 10
U 11
Mvva 196,60,136
E 9
Mava 244,74,136
E 10
Mava 244,78,140
Ta 156,3,29184
C 68,8
Mava 240,78,132
E 11
Mava 240,74,128
LPAD6%=:
s_cbranch_vccnz LRET%=
Ta 158,3,31232
C 69,10
Yvaa 8,96,64,24
U 12
U 13
Mvaa 8,100,68
E 12
Mvaa 8,104,72
U 14
U 15
Mvaa 8,108,76
E 13
Yvaa 124,104,88,24
E 14
Mvaa 124,108,92
Ta 152,3,25088
C 82,12
Mvaa 124,96,80
E 15
Mvaa 124,100,84
s_add_u32 m0, s42, 20480
Ta 154,3,27136
C 83,14
v_smfmac_f32_16x16x64_bf16 a[172:175], a[184:187], v[74:81], v7
buffer_load_dwordx4 v4, s[64:67], s45 offen lds
Mvva 200,88,128
s_xor_b32 s45, s45, 0x1000
U 8
U 9
Mvva 200,92,132
E 8
Mvva 204,92,140
U 10
U 11
Mvva 204,88,136
s_waitcnt vmcnt(7)
E 9
Mava 252,102,136
s_waitcnt lgkmcnt(0)
E 10
Mava 252,106,140
s_barrier
Ba 96,1,0
C 96,8
Mava 248,106,132
E 11
Mava 248,102,128
Ba 100,1,256
C 97,10
Yvaa 8,112,0,16
U 124
U 125
Mvaa 8,116,4
E 124
Mvaa 8,120,8
U 126
U 127
Mvaa 8,124,12
Ba 0,6,0
E 125
Yvaa 12,120,24,16
E 126
Mvaa 12,124,28
Ba 104,1,512
C 110,124
Mvaa 12,112,16
E 127
Mvaa 12,116,20
s_add_u32 m0, s41, 24576
Ba 108,1,768
Ba 4,6,256
C 111,126
v_smfmac_f32_16x16x64_bf16 a[176:179], a[184:187], v[88:95], v7
buffer_load_dwordx4 v4, s[56:59], s43 offen lds
Mvva 208,32,144
U 8
U 9
Mvva 208,36,148
Ba 8,6,512
E 8
Mvva 212,36,156
U 10
U 11
Mvva 212,32,152
buffer_load_dwordx4 v4, s[56:59], s43 offen offset:1024 lds
Ba 12,6,768
E 9
Mvva 220,46,152
s_xor_b32 s43, s43, 0xe000
E 10
Mvva 220,50,156
Ta 130,2,0
C 38,8
Mvva 216,50,148
E 11
Mvva 216,46,144
Ta 128,2,2048
C 39,10
Yvaa 8,112,32,20
U 12
U 13
Mvaa 8,116,36
E 12
Mvaa 8,120,40
U 14
U 15
Mvaa 8,124,44
Ba 16,6,4096
E 13
Yvaa 124,120,56,20
E 14
Mvaa 124,124,60
Ta 134,2,4096
C 52,12
Mvaa 124,112,48
E 15
Mvaa 124,116,52
s_add_u32 m0, s41, 57344
Ta 132,2,6144
Ba 20,6,4352
C 53,14
v_smfmac_f32_16x16x64_bf16 a[180:183], a[184:187], v[102:109], v7
buffer_load_dwordx4 v28, s[60:63], s44 offen lds
Mvva 224,60,144
U 8
U 9
Mvva 224,64,148
Ba 24,6,4608
E 8
Mvva 228,64,156
s_add_u32 m0, s41, 56320
U 10
U 11
Mvva 228,60,152
buffer_load_dwordx4 v28, s[60:63], s44 offen offset:2048 lds
Ba 28,6,4864
E 9
Mvva 236,74,152
s_xor_b32 s44, s44, 0x4080
E 10
Mvva 236,78,156
Ta 142,3,4096
C 66,8
Mvva 232,78,148
E 11
Mvva 232,74,144
Ta 140,3,6144
C 67,10
Yvaa 8,112,64,24
U 124
U 125
Mvaa 8,116,68
E 124
Mvaa 8,120,72
s_add_u32 m0, s42, 21504
U 126
U 127
Mvaa 8,124,76
buffer_load_dwordx4 v4, s[64:67], s45 offen lds
E 125
Yvaa 12,120,88,24
s_xor_b32 s45, s45, 0x3000
E 126
Mvaa 12,124,92
Ta 138,3,0
C 80,124
Mvaa 12,112,80
E 127
Mvaa 12,116,84
Ta 136,3,2048
C 81,126
Mvva 240,88,144
U 8
U 9
Mvva 240,92,148
E 8
Mvva 244,92,156
U 10
U 11
Mvva 244,88,152
E 9
Mvva 252,102,152
s_waitcnt lgkmcnt(0)
E 10
Mvva 252,106,156
Ba 112,1,4096
C 94,8
Mvva 248,106,148
E 11
Mvva 248,102,144
Ba 116,1,4352
C 95,10
LT%=:
s_add_u32 s47, s47, 1
L2x0%=:
Zvaa 8,96,0
Ta 188,30,22528
U 12
U 13
Mvaa 8,100,4
Ta 190,30,22592
E 12
Mvaa 8,104,8
Ba 120,1,4608
U 14
U 15
Mvaa 8,108,12
Ba 124,1,4864
E 13
Zvaa 124,104,24
Ta 158,3,4608
E 14
Mvaa 124,108,28
Ta 156,3,6656
C 108,12
Mvaa 124,96,16
s_waitcnt lgkmcnt(4)
Ta 154,3,512
E 15
Mvaa 124,100,20
buffer_store_dwordx4 a[188:191], v5, s[68:71], s46 offen sc1 nt
C 109,14
v_smfmac_f32_16x16x64_bf16 a[160:163], a[184:187], v[38:45], v7
s_xor_b32 s46, s46, 0x1000
Ta 152,3,2560
v_max3_f32 v116, v8, v9, v10
v_smfmac_f32_16x16x64_bf16 a[164:167], a[184:187], v[52:59], v7
Ta 146,2,512
Mvva 128,42,128
Ta 144,2,2560
Ta 150,2,4608
Mvva 128,38,132
s_barrier
Ta 148,2,6656
v_max3_f32 v116, v116, v11, v124
v_max3_f32 v116, v116, v125, v126
v_max_f32 v117, v116, v127
v_max_f32 v116, v116, v127
Mvva 132,38,140
s_nop 0
v_permlane16_swap_b32 v116, v117
v_max_f32 v116, v116, v117
v_mov_b32 v117, v116
v_accvgpr_read_b32 v118, a160
v_accvgpr_read_b32 v119, a161
v_add_f32 v118, v118, v119
v_rcp_f32 v120, v118
v_accvgpr_write_b32 a160, 0
v_accvgpr_write_b32 a161, 0
v_mul_f32 v119, 0x53800000, v120
v_maximum3_f32 v123, v118, v119, v118
Mvva 132,42,136
s_add_u32 m0, s42, 22528
ds_write_b32 v121, v120 offset:0
Bv 60,31,0
v_permlane32_swap_b32 v116, v117
v_max_f32 v117, v116, v117
v_sub_f32 v16, 0x42db93ec, v117
v_mul_f32 v116, s40, v16
v_pk_fma_f32 v[8:9], v[8:9], s[40:41], v[116:117] op_sel_hi:[1,0,0]
v_pk_fma_f32 v[10:11], v[10:11], s[40:41], v[116:117] op_sel_hi:[1,0,0]
v_mov_b32 v17, v16
v_mov_b32 v18, v16
v_mov_b32 v19, v16
Mvva 140,56,136
buffer_load_dwordx4 v0, s[64:67], s45 offen lds
E 8
Mvva 140,52,140
s_xor_b32 s45, s45, 0x1000
E 9
E 10
Mvva 136,52,132
Ba 96,1,8192
E 11
Mvva 136,56,128
s_waitcnt lgkmcnt(1)
Ba 100,1,8448
C 32,8
v_accvgpr_read_b32 v118, a164
v_accvgpr_read_b32 v119, a165
v_add_f32 v118, v118, v119
v_rcp_f32 v120, v118
v_accvgpr_write_b32 a164, 0
v_accvgpr_write_b32 a165, 0
v_mul_f32 v119, 0x53800000, v120
v_maximum3_f32 v123, v118, v119, v123
v_pk_fma_f32 v[124:125], v[124:125], s[40:41], v[116:117] op_sel_hi:[1,0,0]
v_pk_fma_f32 v[126:127], v[126:127], s[40:41], v[116:117] op_sel_hi:[1,0,0]
v_pk_mul_f32 v[128:129], v[128:129], v[60:61] 
v_pk_mul_f32 v[132:133], v[132:133], v[60:61] 
v_pk_mul_f32 v[130:131], v[130:131], v[62:63] 
v_pk_mul_f32 v[134:135], v[134:135], v[62:63] 
C 33,10
Yvaa 8,112,0,16
ds_write_b32 v121, v120 offset:4096
Bv 74,31,4096
v_cvt_pk_bf16_f32 v128, v128, v132
v_cvt_pk_bf16_f32 v129, v129, v133
Mvaa 8,116,4
Ta 188,30,23552
E 124
Mvaa 8,120,8
Ta 190,30,23616
v_cvt_pk_bf16_f32 v130, v130, v134
v_cvt_pk_bf16_f32 v131, v131, v135
Mvaa 8,124,12
Ba 104,1,8704
Ba 108,1,8960
E 125
Yvaa 12,120,24,16
Ta 142,3,12288
E 126
Mvaa 12,124,28
Ta 140,3,14336
C 46,124
Mvaa 12,112,16
s_waitcnt lgkmcnt(4)
Ta 138,3,8192
E 127
Mvaa 12,116,20
buffer_store_dwordx4 a[188:191], v5, s[68:71], s46 offen sc1 nt
)ASM"
R"ASM(
ds_write_b128 v29, v[128:131] offset:0
v_pk_mul_f32 v[8:9], v[8:9], s[40:41] op_sel_hi:[1,0]
v_pk_mul_f32 v[10:11], v[10:11], s[40:41] op_sel_hi:[1,0]
v_pk_mul_f32 v[136:137], v[136:137], v[74:75] 
v_pk_mul_f32 v[140:141], v[140:141], v[74:75] 
v_pk_mul_f32 v[138:139], v[138:139], v[76:77] 
v_pk_mul_f32 v[142:143], v[142:143], v[76:77] 
C 47,126
Mvva 160,42,144
s_xor_b32 s46, s46, 0x7000
Ta 136,3,10240
v_cvt_pk_bf16_f32 v136, v136, v140
v_cvt_pk_bf16_f32 v137, v137, v141
Mvva 160,38,148
s_add_u32 s46, s46, 0x2f0000
Ta 130,2,8192
E 8
Mvva 164,38,156
Ta 128,2,10240
v_cvt_pk_bf16_f32 v138, v138, v142
v_cvt_pk_bf16_f32 v139, v139, v143
Mvva 164,42,152
Ta 134,2,12288
E 9
Mava 212,56,152
Ta 132,2,14336
E 10
Mava 212,52,156
Ba 112,1,12288
C 34,8
Mava 208,52,148
s_add_u32 m0, s42, 23552
E 11
Mava 208,56,144
s_waitcnt lgkmcnt(10)
ds_write_b128 v29, v[136:139] offset:4096
buffer_load_dwordx4 v0, s[64:67], s45 offen lds
Ba 116,1,12544
v_pk_mul_f32 v[12:13], v[12:13], s[40:41] op_sel_hi:[1,0]
v_pk_mul_f32 v[14:15], v[14:15], s[40:41] op_sel_hi:[1,0]
v_pk_mul_f32 v[160:161], v[160:161], v[60:61] 
v_pk_mul_f32 v[164:165], v[164:165], v[60:61] 
v_pk_mul_f32 v[162:163], v[162:163], v[62:63] 
v_pk_mul_f32 v[166:167], v[166:167], v[62:63] 
C 35,10
Yvaa 8,96,0,16
s_xor_b32 s45, s45, 0x7000
Ba 120,1,12800
v_cvt_pk_bf16_f32 v160, v160, v164
v_cvt_pk_bf16_f32 v161, v161, v165
Mvaa 8,100,4
s_add_u32 s45, s45, 0x2f0000
E 12
Mvaa 8,104,8
s_cmp_eq_u32 s47, 3
v_cvt_pk_bf16_f32 v162, v162, v166
v_cvt_pk_bf16_f32 v163, v163, v167
Mvaa 8,108,12
s_cselect_b32 s66, 0, s66
Ba 124,1,13056
Ba 32,6,8192
E 13
v_accvgpr_read_b32 v128, a208
v_accvgpr_read_b32 v129, a209
v_accvgpr_read_b32 v130, a210
v_accvgpr_read_b32 v131, a211
Yvaa 124,104,24,16
Ta 158,3,12800
E 14
Mvaa 124,108,28
Ba 36,6,8448
C 48,12
Mvaa 124,96,16
Ta 156,3,14848
E 15
Mvaa 124,100,20
s_waitcnt lgkmcnt(9)
ds_write_b128 v29, v[160:163] offset:256
Ba 40,6,8704
v_accvgpr_read_b32 v132, a212
v_accvgpr_read_b32 v133, a213
v_accvgpr_read_b32 v134, a214
v_accvgpr_read_b32 v135, a215
v_pk_mul_f32 v[8:9], v[8:9], s[40:41] op_sel_hi:[1,0]
v_pk_mul_f32 v[10:11], v[10:11], s[40:41] op_sel_hi:[1,0]
v_pk_mul_f32 v[128:129], v[128:129], v[74:75] 
v_pk_mul_f32 v[132:133], v[132:133], v[74:75] 
v_pk_mul_f32 v[130:131], v[130:131], v[76:77] 
v_pk_mul_f32 v[134:135], v[134:135], v[76:77] 
C 49,14
Mvva 184,42,128
Ta 154,3,8704
v_cvt_pk_bf16_f32 v128, v128, v132
v_cvt_pk_bf16_f32 v129, v129, v133
Mvva 184,38,132
Ba 44,6,8960
Ta 152,3,10752
E 8
Mvva 188,38,140
Ta 146,2,8704
v_cvt_pk_bf16_f32 v130, v130, v134
v_cvt_pk_bf16_f32 v131, v131, v135
Mvva 188,42,136
Ta 144,2,10752
Ta 150,2,12800
E 9
Mava 236,56,136
Ta 148,2,14848
E 10
Mava 236,52,140
Ba 96,1,0
C 36,8
Mava 232,52,132
E 11
Mava 232,56,128
s_waitcnt lgkmcnt(6)
ds_write_b128 v29, v[128:131] offset:4352
Ba 100,1,256
v_pk_mul_f32 v[124:125], v[124:125], s[40:41] op_sel_hi:[1,0]
v_pk_mul_f32 v[126:127], v[126:127], s[40:41] op_sel_hi:[1,0]
v_pk_mul_f32 v[184:185], v[184:185], v[60:61] 
v_pk_mul_f32 v[188:189], v[188:189], v[60:61] 
v_pk_mul_f32 v[186:187], v[186:187], v[62:63] 
v_pk_mul_f32 v[190:191], v[190:191], v[62:63] 
C 37,10
Yvaa 8,112,0,16
Ba 104,1,512
v_cvt_pk_bf16_f32 v184, v184, v188
v_cvt_pk_bf16_f32 v185, v185, v189
Mvaa 8,116,4
E 124
Mvaa 8,120,8
v_cvt_pk_bf16_f32 v186, v186, v190
v_cvt_pk_bf16_f32 v187, v187, v191
Mvaa 8,124,12
Ba 108,1,768
Ba 48,6,12288
E 125
v_accvgpr_read_b32 v160, a232
v_accvgpr_read_b32 v161, a233
v_accvgpr_read_b32 v162, a234
v_accvgpr_read_b32 v163, a235
Yvaa 12,120,24,16
Tv 142,3,4096
E 126
Mvaa 12,124,28
Ba 52,6,12544
C 50,124
Mvaa 12,112,16
Tv 140,3,6144
E 127
Mvaa 12,116,20
s_waitcnt lgkmcnt(9)
ds_write_b128 v29, v[184:187] offset:512
Ba 56,6,12800
v_accvgpr_read_b32 v164, a236
v_accvgpr_read_b32 v165, a237
v_accvgpr_read_b32 v166, a238
v_accvgpr_read_b32 v167, a239
v_pk_mul_f32 v[8:9], v[8:9], s[40:41] op_sel_hi:[1,0]
v_pk_mul_f32 v[10:11], v[10:11], s[40:41] op_sel_hi:[1,0]
v_pk_mul_f32 v[160:161], v[160:161], v[74:75] 
v_pk_mul_f32 v[164:165], v[164:165], v[74:75] 
v_pk_mul_f32 v[162:163], v[162:163], v[76:77] 
v_pk_mul_f32 v[166:167], v[166:167], v[76:77] 
C 51,126
Mvva 208,42,144
Tv 138,3,0
v_cvt_pk_bf16_f32 v160, v160, v164
v_cvt_pk_bf16_f32 v161, v161, v165
Mvva 208,38,148
Ba 60,6,13056
Tv 136,3,2048
E 8
Mvva 212,38,156
Tv 130,2,0
v_cvt_pk_bf16_f32 v162, v162, v166
v_cvt_pk_bf16_f32 v163, v163, v167
Mvva 212,42,152
Tv 128,2,2048
Tv 134,2,4096
E 9
Mvva 220,56,152
Tv 132,2,6144
E 10
Mvva 220,52,156
Ba 128,1,4096
C 38,8
Mvva 216,52,148
E 11
Mvva 216,56,144
s_waitcnt lgkmcnt(6)
ds_write_b128 v29, v[160:163] offset:4608
Ba 132,1,4352
v_pk_mul_f32 v[12:13], v[12:13], s[40:41] op_sel_hi:[1,0]
v_pk_mul_f32 v[14:15], v[14:15], s[40:41] op_sel_hi:[1,0]
v_pk_mul_f32 v[208:209], v[208:209], v[60:61] 
v_pk_mul_f32 v[212:213], v[212:213], v[60:61] 
v_pk_mul_f32 v[210:211], v[210:211], v[62:63] 
v_pk_mul_f32 v[214:215], v[214:215], v[62:63] 
C 39,10
Zvaa 8,96,32
s_mov_b32 s70, 0x6000000
Ba 136,1,4608
v_cvt_pk_bf16_f32 v208, v208, v212
v_cvt_pk_bf16_f32 v209, v209, v213
Mvaa 8,100,36
s_cmp_eq_u32 s47, 3
E 12
Mvaa 8,104,40
s_cselect_b32 s52, 0, 0x200000
v_cvt_pk_bf16_f32 v210, v210, v214
v_cvt_pk_bf16_f32 v211, v211, v215
Mvaa 8,108,44
Ba 140,1,4864
E 13
Zvaa 124,104,56
Ta 214,3,4608
E 14
Mvaa 124,108,60
Ta 212,3,6656
C 52,12
Mvaa 124,96,48
Ta 210,3,512
E 15
Mvaa 124,100,52
s_waitcnt lgkmcnt(8)
ds_write_b128 v29, v[208:211] offset:768
v_pk_mul_f32 v[216:217], v[216:217], v[74:75] 
v_pk_mul_f32 v[220:221], v[220:221], v[74:75] 
v_pk_mul_f32 v[218:219], v[218:219], v[76:77] 
v_pk_mul_f32 v[222:223], v[222:223], v[76:77] 
C 53,14
v_smfmac_f32_16x16x64_bf16 a[168:171], a[184:187], v[66:73], v7
Ta 208,3,2560
v_max3_f32 v116, v8, v9, v10
v_smfmac_f32_16x16x64_bf16 a[172:175], a[184:187], v[80:87], v7
Tv 162,2,512
Mvvv 144,70,128
Tv 160,2,2560
v_cvt_pk_bf16_f32 v216, v216, v220
v_cvt_pk_bf16_f32 v217, v217, v221
Mvvv 144,66,132
Tv 166,2,4608
v_max3_f32 v116, v116, v11, v124
v_max3_f32 v116, v116, v125, v126
v_max_f32 v117, v116, v127
v_max_f32 v116, v116, v127
Mvvv 148,66,140
Tv 164,2,6656
v_permlane16_swap_b32 v116, v117
v_max_f32 v116, v116, v117
v_mov_b32 v117, v116
v_cvt_pk_bf16_f32 v218, v218, v222
v_cvt_pk_bf16_f32 v219, v219, v223
v_accvgpr_read_b32 v118, a168
v_accvgpr_read_b32 v119, a169
v_add_f32 v118, v118, v119
v_rcp_f32 v120, v118
v_accvgpr_write_b32 a168, 0
v_accvgpr_write_b32 a169, 0
v_mul_f32 v119, 0x53800000, v120
v_maximum3_f32 v123, v118, v119, v123
Mvvv 148,70,136
LPAD7%=:
s_cbranch_vccnz LRET%=
ds_write_b32 v121, v120 offset:8192
Bv 88,31,8192
v_permlane32_swap_b32 v116, v117
v_max_f32 v117, v116, v117
v_sub_f32 v20, 0x42db93ec, v117
v_mul_f32 v116, s40, v20
v_pk_fma_f32 v[8:9], v[8:9], s[40:41], v[116:117] op_sel_hi:[1,0,0]
v_pk_fma_f32 v[10:11], v[10:11], s[40:41], v[116:117] op_sel_hi:[1,0,0]
v_mov_b32 v21, v20
v_mov_b32 v22, v20
v_mov_b32 v23, v20
Mavv 196,84,136
E 8
Mavv 196,80,140
Ba 112,1,8192
E 9
E 10
Mavv 192,80,132
E 11
Mavv 192,84,128
s_waitcnt lgkmcnt(1)
ds_write_b128 v29, v[216:219] offset:4864
Ba 116,1,8448
C 60,8
v_accvgpr_read_b32 v118, a172
v_accvgpr_read_b32 v119, a173
v_add_f32 v118, v118, v119
v_rcp_f32 v120, v118
v_accvgpr_write_b32 a172, 0
v_accvgpr_write_b32 a173, 0
v_mul_f32 v119, 0x53800000, v120
v_maximum3_f32 v123, v118, v119, v123
v_pk_fma_f32 v[124:125], v[124:125], s[40:41], v[116:117] op_sel_hi:[1,0,0]
v_pk_fma_f32 v[126:127], v[126:127], s[40:41], v[116:117] op_sel_hi:[1,0,0]
v_pk_mul_f32 v[144:145], v[144:145], v[88:89] 
v_pk_mul_f32 v[148:149], v[148:149], v[88:89] 
v_pk_mul_f32 v[146:147], v[146:147], v[90:91] 
v_pk_mul_f32 v[150:151], v[150:151], v[90:91] 
C 61,10
Yvaa 8,128,32,20
ds_write_b32 v121, v120 offset:12288
Bv 102,31,12288
v_cvt_pk_bf16_f32 v144, v144, v148
v_cvt_pk_bf16_f32 v145, v145, v149
Mvaa 8,132,36
Ta 188,30,0
E 124
Mvaa 8,136,40
Ta 190,30,64
v_cvt_pk_bf16_f32 v146, v146, v150
v_cvt_pk_bf16_f32 v147, v147, v151
Mvaa 8,140,44
Ba 120,1,8704
Ba 124,1,8960
E 125
v_accvgpr_read_b32 v208, a192
v_accvgpr_read_b32 v209, a193
v_accvgpr_read_b32 v210, a194
v_accvgpr_read_b32 v211, a195
Yvaa 12,136,56,20
Ta 238,3,12288
E 126
Mvaa 12,140,60
Ta 236,3,14336
C 74,124
Mvaa 12,128,48
s_waitcnt lgkmcnt(4)
Ta 234,3,8192
E 127
Mvaa 12,132,52
buffer_store_dwordx4 a[188:191], v5, s[68:71], s46 offen sc1 nt
ds_write_b128 v29, v[144:147] offset:8192
v_accvgpr_read_b32 v212, a196
v_accvgpr_read_b32 v213, a197
v_accvgpr_read_b32 v214, a198
v_accvgpr_read_b32 v215, a199
v_pk_mul_f32 v[8:9], v[8:9], s[40:41] op_sel_hi:[1,0]
v_pk_mul_f32 v[10:11], v[10:11], s[40:41] op_sel_hi:[1,0]
v_pk_mul_f32 v[208:209], v[208:209], v[102:103] 
v_pk_mul_f32 v[212:213], v[212:213], v[102:103] 
v_pk_mul_f32 v[210:211], v[210:211], v[104:105] 
v_pk_mul_f32 v[214:215], v[214:215], v[104:105] 
C 75,126
Mvvv 168,70,160
s_xor_b32 s46, s46, 0x1000
Ta 232,3,10240
v_cvt_pk_bf16_f32 v208, v208, v212
v_cvt_pk_bf16_f32 v209, v209, v213
Mvvv 168,66,164
Tv 186,2,8192
E 8
Mvva 172,66,212
Tv 184,2,10240
v_cvt_pk_bf16_f32 v210, v210, v214
v_cvt_pk_bf16_f32 v211, v211, v215
Mvva 172,70,208
Tv 190,2,12288
E 9
Mava 220,84,208
Tv 188,2,14336
E 10
Mava 220,80,212
Ba 144,1,12288
C 62,8
Mavv 216,80,164
s_add_u32 m0, s42, 0
E 11
Mavv 216,84,160
s_waitcnt lgkmcnt(10)
ds_write_b128 v29, v[208:211] offset:12288
buffer_load_dwordx4 v4, s[64:67], s45 offen lds
Ba 148,1,12544
v_pk_mul_f32 v[12:13], v[12:13], s[40:41] op_sel_hi:[1,0]
v_pk_mul_f32 v[14:15], v[14:15], s[40:41] op_sel_hi:[1,0]
v_pk_mul_f32 v[168:169], v[168:169], v[88:89] 
v_pk_mul_f32 v[172:173], v[172:173], v[88:89] 
v_pk_mul_f32 v[170:171], v[170:171], v[90:91] 
v_pk_mul_f32 v[174:175], v[174:175], v[90:91] 
C 63,10
Yvaa 8,112,32,20
s_xor_b32 s45, s45, 0x1000
s_waitcnt vmcnt(12)
Ta 188,30,1024
v_cvt_pk_bf16_f32 v168, v168, v172
v_cvt_pk_bf16_f32 v169, v169, v173
Mvaa 8,116,36
Ta 190,30,1088
E 12
Mvaa 8,120,40
Ba 152,1,12800
v_cvt_pk_bf16_f32 v170, v170, v174
v_cvt_pk_bf16_f32 v171, v171, v175
Mvaa 8,124,44
Ba 156,1,13056
)ASM"
R"ASM(
Ba 64,6,16384
E 13
v_accvgpr_read_b32 v144, a216
v_accvgpr_read_b32 v145, a217
v_accvgpr_read_b32 v146, a218
v_accvgpr_read_b32 v147, a219
Yvaa 124,120,56,20
Tv 222,3,12800
Ba 68,6,16640
E 14
Mvaa 124,124,60
Tv 220,3,14848
C 76,12
Mvaa 124,112,48
s_waitcnt lgkmcnt(6)
Ba 72,6,16896
E 15
Mvaa 124,116,52
buffer_store_dwordx4 a[188:191], v5, s[68:71], s46 offen sc1 nt
ds_write_b128 v29, v[168:171] offset:8448
v_accvgpr_read_b32 v148, a220
v_accvgpr_read_b32 v149, a221
v_accvgpr_read_b32 v150, a222
v_accvgpr_read_b32 v151, a223
v_pk_mul_f32 v[8:9], v[8:9], s[40:41] op_sel_hi:[1,0]
v_pk_mul_f32 v[10:11], v[10:11], s[40:41] op_sel_hi:[1,0]
v_pk_mul_f32 v[144:145], v[144:145], v[102:103] 
v_pk_mul_f32 v[148:149], v[148:149], v[102:103] 
v_pk_mul_f32 v[146:147], v[146:147], v[104:105] 
v_pk_mul_f32 v[150:151], v[150:151], v[104:105] 
C 77,14
Mvvv 192,70,184
s_xor_b32 s46, s46, 0x3000
Tv 218,3,8704
v_cvt_pk_bf16_f32 v144, v144, v148
v_cvt_pk_bf16_f32 v145, v145, v149
Mvvv 192,66,188
Ba 76,6,17152
Tv 216,3,10752
E 8
Mvva 196,66,236
Tv 210,2,8704
v_cvt_pk_bf16_f32 v146, v146, v150
v_cvt_pk_bf16_f32 v147, v147, v151
Mvva 196,70,232
Tv 208,2,10752
Tv 214,2,12800
E 9
Mava 244,84,232
s_add_u32 m0, s42, 1024
Tv 212,2,14848
E 10
Mava 244,80,236
buffer_load_dwordx4 v4, s[64:67], s45 offen lds
C 64,8
Mavv 240,80,188
s_xor_b32 s45, s45, 0x3000
E 11
Mavv 240,84,184
s_waitcnt lgkmcnt(5)
ds_write_b128 v29, v[144:147] offset:12544
v_pk_mul_f32 v[124:125], v[124:125], s[40:41] op_sel_hi:[1,0]
v_pk_mul_f32 v[126:127], v[126:127], s[40:41] op_sel_hi:[1,0]
v_pk_mul_f32 v[192:193], v[192:193], v[88:89] 
v_pk_mul_f32 v[196:197], v[196:197], v[88:89] 
v_pk_mul_f32 v[194:195], v[194:195], v[90:91] 
v_pk_mul_f32 v[198:199], v[198:199], v[90:91] 
C 65,10
Yvaa 8,144,32,20
s_waitcnt vmcnt(4)
Ta 188,30,2048
v_cvt_pk_bf16_f32 v192, v192, v196
v_cvt_pk_bf16_f32 v193, v193, v197
Mvaa 8,148,36
Ta 190,30,2112
E 124
Mvaa 8,152,40
Ba 80,6,20480
v_cvt_pk_bf16_f32 v194, v194, v198
v_cvt_pk_bf16_f32 v195, v195, v199
Mvaa 8,156,44
Ba 84,6,20736
E 125
v_accvgpr_read_b32 v168, a240
v_accvgpr_read_b32 v169, a241
v_accvgpr_read_b32 v170, a242
v_accvgpr_read_b32 v171, a243
Yvaa 12,152,56,20
E 126
Mvaa 12,156,60
Ba 88,6,20992
C 78,124
Mvaa 12,144,48
s_waitcnt lgkmcnt(3)
E 127
Mvaa 12,148,52
buffer_store_dwordx4 a[188:191], v5, s[68:71], s46 offen sc1 nt
ds_write_b128 v29, v[192:195] offset:8704
v_accvgpr_read_b32 v172, a244
v_accvgpr_read_b32 v173, a245
v_accvgpr_read_b32 v174, a246
v_accvgpr_read_b32 v175, a247
v_pk_mul_f32 v[8:9], v[8:9], s[40:41] op_sel_hi:[1,0]
v_pk_mul_f32 v[10:11], v[10:11], s[40:41] op_sel_hi:[1,0]
v_pk_mul_f32 v[168:169], v[168:169], v[102:103] 
v_pk_mul_f32 v[172:173], v[172:173], v[102:103] 
v_pk_mul_f32 v[170:171], v[170:171], v[104:105] 
v_pk_mul_f32 v[174:175], v[174:175], v[104:105] 
C 79,126
Mvvv 224,70,208
s_xor_b32 s46, s46, 0x1000
v_cvt_pk_bf16_f32 v168, v168, v172
v_cvt_pk_bf16_f32 v169, v169, v173
Mvvv 224,66,212
s_barrier
Ba 92,6,21248
E 8
Mvvv 228,66,220
v_cvt_pk_bf16_f32 v170, v170, v174
v_cvt_pk_bf16_f32 v171, v171, v175
Mvvv 228,70,216
E 9
Mvvv 236,84,216
s_add_u32 m0, s42, 2048
E 10
Mvvv 236,80,220
buffer_load_dwordx4 v0, s[64:67], s45 offen lds
C 66,8
Mvvv 232,80,212
s_xor_b32 s45, s45, 0x1000
E 11
Mvvv 232,84,208
s_waitcnt lgkmcnt(0)
ds_write_b128 v29, v[168:171] offset:12800
v_pk_mul_f32 v[12:13], v[12:13], s[40:41] op_sel_hi:[1,0]
v_pk_mul_f32 v[14:15], v[14:15], s[40:41] op_sel_hi:[1,0]
v_pk_mul_f32 v[224:225], v[224:225], v[88:89] 
v_pk_mul_f32 v[228:229], v[228:229], v[88:89] 
v_pk_mul_f32 v[226:227], v[226:227], v[90:91] 
v_pk_mul_f32 v[230:231], v[230:231], v[90:91] 
C 67,10
Zvaa 8,96,64
Ta 188,30,3072
v_cvt_pk_bf16_f32 v224, v224, v228
v_cvt_pk_bf16_f32 v225, v225, v229
Mvaa 8,100,68
E 12
Mvaa 8,104,72
v_cvt_pk_bf16_f32 v226, v226, v230
v_cvt_pk_bf16_f32 v227, v227, v231
Mvaa 8,108,76
Ta 190,30,3136
E 13
Zvaa 124,104,88
E 14
Mvaa 124,108,92
C 80,12
Mvaa 124,96,80
s_waitcnt lgkmcnt(0)
E 15
Mvaa 124,100,84
s_add_u32 m0, s42, 3072
buffer_store_dwordx4 a[188:191], v5, s[68:71], s46 offen sc1 nt
ds_write_b128 v29, v[224:227] offset:8960
v_pk_mul_f32 v[232:233], v[232:233], v[102:103] 
v_pk_mul_f32 v[236:237], v[236:237], v[102:103] 
v_pk_mul_f32 v[234:235], v[234:235], v[104:105] 
v_pk_mul_f32 v[238:239], v[238:239], v[104:105] 
C 81,14
v_smfmac_f32_16x16x64_bf16 a[176:179], a[184:187], v[94:101], v7
s_xor_b32 s46, s46, 0x7000
v_max3_f32 v116, v8, v9, v10
v_smfmac_f32_16x16x64_bf16 a[180:183], a[184:187], v[108:115], v7
buffer_load_dwordx4 v0, s[64:67], s45 offen lds
Mvvv 152,98,128
s_xor_b32 s45, s45, 0x7000
v_cvt_pk_bf16_f32 v232, v232, v236
v_cvt_pk_bf16_f32 v233, v233, v237
Mvvv 152,94,132
v_max3_f32 v116, v116, v11, v124
v_max3_f32 v116, v116, v125, v126
v_max_f32 v117, v116, v127
v_max_f32 v116, v116, v127
Mvvv 156,94,140
s_nop 0
v_permlane16_swap_b32 v116, v117
v_max_f32 v116, v116, v117
v_mov_b32 v117, v116
v_cvt_pk_bf16_f32 v234, v234, v238
v_cvt_pk_bf16_f32 v235, v235, v239
v_accvgpr_read_b32 v118, a176
v_accvgpr_read_b32 v119, a177
v_add_f32 v118, v118, v119
v_rcp_f32 v120, v118
v_accvgpr_write_b32 a176, 0
v_accvgpr_write_b32 a177, 0
v_mul_f32 v119, 0x53800000, v120
v_maximum3_f32 v123, v118, v119, v123
Mvvv 156,98,136
ds_write_b32 v121, v120 offset:16384
Bv 68,31,16384
v_permlane32_swap_b32 v116, v117
v_max_f32 v117, v116, v117
v_sub_f32 v24, 0x42db93ec, v117
v_mul_f32 v116, s40, v24
v_pk_fma_f32 v[8:9], v[8:9], s[40:41], v[116:117] op_sel_hi:[1,0,0]
v_pk_fma_f32 v[10:11], v[10:11], s[40:41], v[116:117] op_sel_hi:[1,0,0]
v_mov_b32 v25, v24
v_mov_b32 v26, v24
v_mov_b32 v27, v24
Mavv 204,112,136
E 8
Mavv 204,108,140
E 9
E 10
Mavv 200,108,132
E 11
Mavv 200,112,128
s_waitcnt lgkmcnt(0)
ds_write_b128 v29, v[232:235] offset:13056
C 88,8
v_accvgpr_read_b32 v118, a180
v_accvgpr_read_b32 v119, a181
v_add_f32 v118, v118, v119
v_rcp_f32 v120, v118
v_accvgpr_write_b32 a180, 0
v_accvgpr_write_b32 a181, 0
v_mul_f32 v119, 0x53800000, v120
v_maximum3_f32 v123, v118, v119, v123
v_cmp_nle_f32 s[48:49], v123, s51
v_pk_fma_f32 v[124:125], v[124:125], s[40:41], v[116:117] op_sel_hi:[1,0,0]
v_pk_fma_f32 v[126:127], v[126:127], s[40:41], v[116:117] op_sel_hi:[1,0,0]
v_pk_mul_f32 v[152:153], v[152:153], v[68:69] 
v_pk_mul_f32 v[156:157], v[156:157], v[68:69] 
v_pk_mul_f32 v[154:155], v[154:155], v[70:71] 
v_pk_mul_f32 v[158:159], v[158:159], v[70:71] 
C 89,10
Yvaa 8,128,64,24
ds_write_b32 v121, v120 offset:20480
Bv 82,31,20480
v_cvt_pk_bf16_f32 v152, v152, v156
v_cvt_pk_bf16_f32 v153, v153, v157
Mvaa 8,132,68
Ta 188,30,4096
E 124
Mvaa 8,136,72
Ta 190,30,4160
v_cvt_pk_bf16_f32 v154, v154, v158
v_cvt_pk_bf16_f32 v155, v155, v159
Mvaa 8,140,76
Ba 96,1,16384
Ba 100,1,16640
E 125
v_accvgpr_read_b32 v224, a200
v_accvgpr_read_b32 v225, a201
v_accvgpr_read_b32 v226, a202
v_accvgpr_read_b32 v227, a203
Yvaa 12,136,88,24
E 126
Mvaa 12,140,92
Ba 104,1,16896
C 102,124
Mvaa 12,128,80
s_waitcnt lgkmcnt(3)
E 127
Mvaa 12,132,84
buffer_store_dwordx4 a[188:191], v5, s[68:71], s46 offen sc1 nt
ds_write_b128 v29, v[152:155] offset:16384
v_accvgpr_read_b32 v228, a204
v_accvgpr_read_b32 v229, a205
v_accvgpr_read_b32 v230, a206
v_accvgpr_read_b32 v231, a207
v_pk_mul_f32 v[8:9], v[8:9], s[40:41] op_sel_hi:[1,0]
v_pk_mul_f32 v[10:11], v[10:11], s[40:41] op_sel_hi:[1,0]
v_pk_mul_f32 v[224:225], v[224:225], v[82:83] 
v_pk_mul_f32 v[228:229], v[228:229], v[82:83] 
v_pk_mul_f32 v[226:227], v[226:227], v[84:85] 
v_pk_mul_f32 v[230:231], v[230:231], v[84:85] 
C 103,126
Mvvv 176,98,160
s_xor_b32 s46, s46, 0x1000
v_cvt_pk_bf16_f32 v224, v224, v228
v_cvt_pk_bf16_f32 v225, v225, v229
Mvvv 176,94,164
Ba 108,1,17152
E 8
Mvva 180,94,212
v_cvt_pk_bf16_f32 v226, v226, v230
v_cvt_pk_bf16_f32 v227, v227, v231
Mvva 180,98,208
E 9
Mava 228,112,208
s_add_u32 m0, s42, 4096
E 10
Mava 228,108,212
buffer_load_dwordx4 v4, s[64:67], s45 offen lds
C 90,8
Mavv 224,108,164
s_xor_b32 s45, s45, 0x1000
E 11
Mavv 224,112,160
ds_write_b128 v29, v[224:227] offset:20480
v_pk_mul_f32 v[12:13], v[12:13], s[40:41] op_sel_hi:[1,0]
v_pk_mul_f32 v[14:15], v[14:15], s[40:41] op_sel_hi:[1,0]
v_pk_mul_f32 v[176:177], v[176:177], v[68:69] 
v_pk_mul_f32 v[180:181], v[180:181], v[68:69] 
v_pk_mul_f32 v[178:179], v[178:179], v[70:71] 
v_pk_mul_f32 v[182:183], v[182:183], v[70:71] 
C 91,10
Yvaa 8,112,64,24
Ta 188,30,5120
v_cvt_pk_bf16_f32 v176, v176, v180
v_cvt_pk_bf16_f32 v177, v177, v181
Mvaa 8,116,68
Ta 190,30,5184
E 12
Mvaa 8,120,72
Ta 128,2,16384
v_cvt_pk_bf16_f32 v178, v178, v182
v_cvt_pk_bf16_f32 v179, v179, v183
Mvaa 8,124,76
Ta 130,2,18432
E 13
v_accvgpr_read_b32 v152, a224
v_accvgpr_read_b32 v153, a225
v_accvgpr_read_b32 v154, a226
v_accvgpr_read_b32 v155, a227
Yvaa 124,120,88,24
Ta 132,2,20480
E 14
Mvaa 124,124,92
Ta 134,2,22528
C 104,12
Mvaa 124,112,80
s_waitcnt lgkmcnt(4)
E 15
Mvaa 124,116,84
buffer_store_dwordx4 a[188:191], v5, s[68:71], s46 offen sc1 nt
ds_write_b128 v29, v[176:179] offset:16640
v_accvgpr_read_b32 v156, a228
v_accvgpr_read_b32 v157, a229
v_accvgpr_read_b32 v158, a230
v_accvgpr_read_b32 v159, a231
v_pk_mul_f32 v[8:9], v[8:9], s[40:41] op_sel_hi:[1,0]
v_pk_mul_f32 v[10:11], v[10:11], s[40:41] op_sel_hi:[1,0]
v_pk_mul_f32 v[152:153], v[152:153], v[82:83] 
v_pk_mul_f32 v[156:157], v[156:157], v[82:83] 
v_pk_mul_f32 v[154:155], v[154:155], v[84:85] 
v_pk_mul_f32 v[158:159], v[158:159], v[84:85] 
C 105,14
Mvvv 200,98,184
s_xor_b32 s46, s46, 0x3000
v_cvt_pk_bf16_f32 v152, v152, v156
v_cvt_pk_bf16_f32 v153, v153, v157
Mvvv 200,94,188
Ta 140,3,20480
E 8
Mvva 204,94,236
v_cvt_pk_bf16_f32 v154, v154, v158
v_cvt_pk_bf16_f32 v155, v155, v159
Mvva 204,98,232
Ta 142,3,22528
E 9
Mava 252,112,232
s_add_u32 m0, s42, 5120
E 10
Mava 252,108,236
buffer_load_dwordx4 v4, s[64:67], s45 offen lds
C 92,8
Mavv 248,108,188
s_xor_b32 s45, s45, 0x3000
E 11
Mavv 248,112,184
ds_write_b128 v29, v[152:155] offset:20736
v_pk_mul_f32 v[124:125], v[124:125], s[40:41] op_sel_hi:[1,0]
v_pk_mul_f32 v[126:127], v[126:127], s[40:41] op_sel_hi:[1,0]
v_pk_mul_f32 v[200:201], v[200:201], v[68:69] 
v_pk_mul_f32 v[204:205], v[204:205], v[68:69] 
v_pk_mul_f32 v[202:203], v[202:203], v[70:71] 
v_pk_mul_f32 v[206:207], v[206:207], v[70:71] 
C 93,10
Yvaa 8,144,64,24
s_lshl_b64 s[54:55], s[54:55], 16
Ta 136,3,16384
v_cvt_pk_bf16_f32 v200, v200, v204
v_cvt_pk_bf16_f32 v201, v201, v205
Mvaa 8,148,68
s_and_b32 s53, s48, 0xffff
E 124
Mvaa 8,152,72
s_or_b32 s54, s54, s53
v_cvt_pk_bf16_f32 v202, v202, v206
v_cvt_pk_bf16_f32 v203, v203, v207
Mvaa 8,156,76
Ta 138,3,18432
E 125
v_accvgpr_read_b32 v176, a248
v_accvgpr_read_b32 v177, a249
v_accvgpr_read_b32 v178, a250
v_accvgpr_read_b32 v179, a251
)ASM"
R"ASM(
Yvaa 12,152,88,24
s_add_u32 m0, s41, 0
E 126
Mvaa 12,156,92
buffer_load_dwordx4 v4, s[56:59], s43 offen lds
C 106,124
Mvaa 12,144,80
buffer_load_dwordx4 v4, s[56:59], s43 offen offset:1024 lds
E 127
Mvaa 12,148,84
s_xor_b32 s43, s43, 0x2000
ds_write_b128 v29, v[200:203] offset:16896
v_accvgpr_read_b32 v180, a252
v_accvgpr_read_b32 v181, a253
v_accvgpr_read_b32 v182, a254
v_accvgpr_read_b32 v183, a255
v_pk_mul_f32 v[8:9], v[8:9], s[40:41] op_sel_hi:[1,0]
v_pk_mul_f32 v[10:11], v[10:11], s[40:41] op_sel_hi:[1,0]
v_pk_mul_f32 v[176:177], v[176:177], v[82:83] 
v_pk_mul_f32 v[180:181], v[180:181], v[82:83] 
v_pk_mul_f32 v[178:179], v[178:179], v[84:85] 
v_pk_mul_f32 v[182:183], v[182:183], v[84:85] 
C 107,126
Mvvv 240,98,208
s_add_u32 m0, s41, 32768
v_cvt_pk_bf16_f32 v176, v176, v180
v_cvt_pk_bf16_f32 v177, v177, v181
Mvvv 240,94,212
LPAD8%=:
s_cbranch_vccnz LRET%=
buffer_load_dwordx4 v28, s[60:63], s44 offen lds
E 8
Mvvv 244,94,220
v_cvt_pk_bf16_f32 v178, v178, v182
v_cvt_pk_bf16_f32 v179, v179, v183
Mvvv 244,98,216
s_add_u32 m0, s41, 31744
E 9
Mvvv 252,112,216
buffer_load_dwordx4 v28, s[60:63], s44 offen offset:2048 lds
E 10
Mvvv 252,108,220
s_xor_b32 s44, s44, 0x80
Ba 112,1,20480
C 94,8
Mvvv 248,108,212
E 11
Mvvv 248,112,208
ds_write_b128 v29, v[176:179] offset:20992
Ba 116,1,20736
v_pk_mul_f32 v[12:13], v[12:13], s[40:41] op_sel_hi:[1,0]
v_pk_mul_f32 v[14:15], v[14:15], s[40:41] op_sel_hi:[1,0]
v_pk_mul_f32 v[240:241], v[240:241], v[68:69] 
v_pk_mul_f32 v[244:245], v[244:245], v[68:69] 
v_pk_mul_f32 v[242:243], v[242:243], v[70:71] 
v_pk_mul_f32 v[246:247], v[246:247], v[70:71] 
C 95,10
L2x12%=:
Yvaa 8,96,0,16
v_cvt_pk_bf16_f32 v240, v240, v244
v_cvt_pk_bf16_f32 v241, v241, v245
Mvaa 8,100,4
E 12
Mvaa 8,104,8
v_cvt_pk_bf16_f32 v242, v242, v246
v_cvt_pk_bf16_f32 v243, v243, v247
Mvaa 8,108,12
E 13
Yvaa 124,104,24,16
E 14
Mvaa 124,108,28
Ba 120,1,20992
C 108,12
Mvaa 124,96,16
s_add_u32 m0, s41, 8192
E 15
Mvaa 124,100,20
s_waitcnt lgkmcnt(5)
ds_write_b128 v29, v[240:243] offset:17152
Ba 124,1,21248
v_pk_mul_f32 v[8:9], v[8:9], s[40:41] op_sel_hi:[1,0]
v_pk_mul_f32 v[10:11], v[10:11], s[40:41] op_sel_hi:[1,0]
v_pk_mul_f32 v[248:249], v[248:249], v[82:83] 
v_pk_mul_f32 v[252:253], v[252:253], v[82:83] 
v_pk_mul_f32 v[250:251], v[250:251], v[84:85] 
v_pk_mul_f32 v[254:255], v[254:255], v[84:85] 
C 109,14
v_smfmac_f32_16x16x64_bf16 a[160:163], a[184:187], v[32:39], v7
buffer_load_dwordx4 v4, s[56:59], s43 offen lds
Zvva 128,32,128
v_cvt_pk_bf16_f32 v248, v248, v252
v_cvt_pk_bf16_f32 v249, v249, v253
Mvva 128,36,132
E 8
Zvva 132,36,140
v_cvt_pk_bf16_f32 v250, v250, v254
v_cvt_pk_bf16_f32 v251, v251, v255
Mvva 132,32,136
buffer_load_dwordx4 v4, s[56:59], s43 offen offset:1024 lds
E 9
Zvva 140,46,136
s_xor_b32 s43, s43, 0x6000
E 10
Mvva 140,50,140
Ta 144,2,16896
C 44,8
Zvva 136,50,132
E 11
Mvva 136,46,128
ds_write_b128 v29, v[248:251] offset:21248
Ta 146,2,18944
C 45,10
L2x13%=:
Yvaa 8,96,32,20
U 124
U 125
Mvaa 8,100,36
E 124
Mvaa 8,104,40
U 126
U 127
Mvaa 8,108,44
E 125
Yvaa 12,104,56,20
E 126
Mvaa 12,108,60
Ta 148,2,20992
C 58,124
Mvaa 12,96,48
E 127
Mvaa 12,100,52
s_add_u32 m0, s41, 40960
Ta 150,2,23040
C 59,126
v_smfmac_f32_16x16x64_bf16 a[164:167], a[184:187], v[46:53], v7
buffer_load_dwordx4 v28, s[60:63], s44 offen lds
Zvva 144,60,128
U 8
U 9
Mvva 144,64,132
E 8
Zvva 148,64,140
s_add_u32 m0, s41, 39936
U 10
U 11
Mvva 148,60,136
buffer_load_dwordx4 v28, s[60:63], s44 offen offset:2048 lds
E 9
Zava 196,74,136
s_xor_b32 s44, s44, 0xc080
E 10
Mava 196,78,140
Ta 156,3,20992
C 72,8
Zava 192,78,132
E 11
Mava 192,74,128
Ta 158,3,23040
C 73,10
Yvaa 8,96,64,24
U 12
U 13
Mvaa 8,100,68
E 12
Mvaa 8,104,72
U 14
U 15
Mvaa 8,108,76
E 13
Yvaa 124,104,88,24
E 14
Mvaa 124,108,92
Ta 152,3,16896
C 86,12
Mvaa 124,96,80
E 15
Mvaa 124,100,84
Ta 154,3,18944
C 87,14
Zvva 152,88,128
U 8
U 9
Mvva 152,92,132
E 8
Zvva 156,92,140
U 10
U 11
Mvva 156,88,136
E 9
Zava 204,102,136
s_waitcnt lgkmcnt(0)
E 10
Mava 204,106,140
Ba 96,1,24576
C 100,8
Zava 200,106,132
E 11
Mava 200,102,128
Ba 100,1,24832
C 101,10
Yvaa 8,112,0,16
U 124
U 125
Mvaa 8,116,4
E 124
Mvaa 8,120,8
U 126
U 127
Mvaa 8,124,12
E 125
Yvaa 12,120,24,16
E 126
Mvaa 12,124,28
Ba 104,1,25088
C 114,124
Mvaa 12,112,16
E 127
Mvaa 12,116,20
Ba 108,1,25344
C 115,126
v_smfmac_f32_16x16x64_bf16 a[168:171], a[184:187], v[60:67], v7
Zvva 160,32,144
U 8
U 9
Mvva 160,36,148
E 8
Zvva 164,36,156
U 10
U 11
Mvva 164,32,152
E 9
Zava 212,46,152
E 10
Mava 212,50,156
Ta 128,2,24576
C 42,8
Zava 208,50,148
E 11
Mava 208,46,144
Ta 130,2,26624
C 43,10
Yvaa 8,112,32,20
U 12
U 13
Mvaa 8,116,36
E 12
Mvaa 8,120,40
U 14
U 15
Mvaa 8,124,44
E 13
Yvaa 124,120,56,20
E 14
Mvaa 124,124,60
Ta 132,2,28672
C 56,12
Mvaa 124,112,48
E 15
Mvaa 124,116,52
Ta 134,2,30720
C 57,14
Zvva 168,60,144
U 8
U 9
Mvva 168,64,148
E 8
Zvva 172,64,156
U 10
U 11
Mvva 172,60,152
E 9
Zava 220,74,152
E 10
Mava 220,78,156
Ta 140,3,28672
C 70,8
Zava 216,78,148
E 11
Mava 216,74,144
Ta 142,3,30720
C 71,10
Yvaa 8,112,64,24
U 124
U 125
Mvaa 8,116,68
E 124
Mvaa 8,120,72
U 126
U 127
Mvaa 8,124,76
E 125
Yvaa 12,120,88,24
E 126
Mvaa 12,124,92
Ta 136,3,24576
C 84,124
Mvaa 12,112,80
E 127
Mvaa 12,116,84
Ta 138,3,26624
C 85,126
Zvva 176,88,144
U 8
U 9
Mvva 176,92,148
E 8
Zvva 180,92,156
U 10
U 11
Mvva 180,88,152
E 9
Zava 228,102,152
s_waitcnt lgkmcnt(0)
E 10
Mava 228,106,156
Ba 112,1,28672
C 98,8
Zava 224,106,148
E 11
Mava 224,102,144
Ba 116,1,28928
C 99,10
Yvaa 8,96,0,16
U 12
U 13
Mvaa 8,100,4
E 12
Mvaa 8,104,8
U 14
U 15
Mvaa 8,108,12
E 13
Yvaa 124,104,24,16
E 14
Mvaa 124,108,28
Ba 120,1,29184
C 112,12
Mvaa 124,96,16
E 15
Mvaa 124,100,20
Ba 124,1,29440
C 113,14
Zvva 184,32,128
U 8
U 9
Mvva 184,36,132
E 8
Zvva 188,36,140
U 10
U 11
Mvva 188,32,136
E 9
Zava 236,46,136
E 10
Mava 236,50,140
Ta 144,2,25088
C 40,8
Zava 232,50,132
E 11
Mava 232,46,128
Ta 146,2,27136
C 41,10
Yvaa 8,96,32,20
U 124
U 125
Mvaa 8,100,36
E 124
Mvaa 8,104,40
U 126
U 127
Mvaa 8,108,44
Ta 188,30,6144
Ta 190,30,6208
E 125
Yvaa 12,104,56,20
E 126
Mvaa 12,108,60
Ta 148,2,29184
C 54,124
Mvaa 12,96,48
E 127
Mvaa 12,100,52
Ta 150,2,31232
C 55,126
Zvva 192,60,128
s_waitcnt lgkmcnt(2)
U 8
U 9
Mvva 192,64,132
buffer_store_dwordx4 a[188:191], v5, s[68:71], s46 offen sc1 nt
E 8
U 10
Zvva 196,64,140
s_xor_b32 s46, s46, 0x1000
Tv 116,30,7168
U 11
Mvva 196,60,136
Tv 118,30,7232
E 9
Zava 244,74,136
)ASM"
R"ASM(
E 10
Mava 244,78,140
Ta 156,3,29184
C 68,8
Zava 240,78,132
E 11
Mava 240,74,128
Ta 158,3,31232
C 69,10
Yvaa 8,96,64,24
U 12
U 13
Mvaa 8,100,68
E 12
Mvaa 8,104,72
U 14
U 15
Mvaa 8,108,76
E 13
Yvaa 124,104,88,24
E 14
Mvaa 124,108,92
Ta 152,3,25088
C 82,12
Mvaa 124,96,80
E 15
Mvaa 124,100,84
s_add_u32 m0, s42, 6144
Ta 154,3,27136
C 83,14
v_smfmac_f32_16x16x64_bf16 a[172:175], a[184:187], v[74:81], v7
buffer_load_dwordx4 v0, s[64:67], s45 offen lds
Zvva 200,88,128
s_xor_b32 s45, s45, 0x1000
U 8
U 9
Mvva 200,92,132
E 8
Zvva 204,92,140
U 10
U 11
Mvva 204,88,136
s_waitcnt vmcnt(2)
E 9
Zava 252,102,136
s_waitcnt lgkmcnt(0)
E 10
Mava 252,106,140
s_barrier
Ba 96,1,0
C 96,8
Zava 248,106,132
E 11
Mava 248,102,128
Ba 100,1,256
C 97,10
Yvaa 8,112,0,16
U 124
U 125
Mvaa 8,116,4
E 124
Mvaa 8,120,8
U 126
U 127
Mvaa 8,124,12
E 125
Yvaa 12,120,24,16
E 126
Mvaa 12,124,28
Ba 104,1,512
C 110,124
Mvaa 12,112,16
E 127
Mvaa 12,116,20
s_add_u32 m0, s41, 16384
Ba 108,1,768
C 111,126
v_smfmac_f32_16x16x64_bf16 a[176:179], a[184:187], v[88:95], v7
buffer_load_dwordx4 v4, s[56:59], s43 offen lds
Zvva 208,32,144
U 8
U 9
Mvva 208,36,148
E 8
Zvva 212,36,156
U 10
U 11
Mvva 212,32,152
buffer_load_dwordx4 v4, s[56:59], s43 offen offset:1024 lds
E 9
Zvva 220,46,152
s_xor_b32 s43, s43, 0x2000
E 10
Mvva 220,50,156
Ta 130,2,0
C 38,8
Zvva 216,50,148
E 11
Mvva 216,46,144
Ta 128,2,2048
C 39,10
Yvaa 8,112,32,20
U 12
U 13
Mvaa 8,116,36
E 12
Mvaa 8,120,40
U 14
U 15
Mvaa 8,124,44
E 13
Yvaa 124,120,56,20
E 14
Mvaa 124,124,60
Ta 134,2,4096
C 52,12
Mvaa 124,112,48
E 15
Mvaa 124,116,52
s_add_u32 m0, s41, 49152
Ta 132,2,6144
C 53,14
v_smfmac_f32_16x16x64_bf16 a[180:183], a[184:187], v[102:109], v7
buffer_load_dwordx4 v28, s[60:63], s44 offen lds
Zvva 224,60,144
U 8
U 9
Mvva 224,64,148
E 8
Zvva 228,64,156
s_add_u32 m0, s41, 48128
U 10
U 11
Mvva 228,60,152
buffer_load_dwordx4 v28, s[60:63], s44 offen offset:2048 lds
E 9
Zvva 236,74,152
s_xor_b32 s44, s44, 0x80
E 10
Mvva 236,78,156
Ta 142,3,4096
C 66,8
Zvva 232,78,148
E 11
Mvva 232,74,144
Ta 140,3,6144
C 67,10
Yvaa 8,112,64,24
U 124
U 125
Mvaa 8,116,68
E 124
Mvaa 8,120,72
U 126
U 127
Mvaa 8,124,76
E 125
Yvaa 12,120,88,24
E 126
Mvaa 12,124,92
Ta 138,3,0
C 80,124
Mvaa 12,112,80
E 127
Mvaa 12,116,84
Ta 136,3,2048
C 81,126
v_add_u32 v120, L2x36%=-LPF%=, v122
Zvva 240,88,144
U 8
U 9
Mvva 240,92,148
buffer_load_dword v120, v120, s[72:75], 0 offen
E 8
Zvva 244,92,156
U 10
U 11
Mvva 244,88,152
E 9
Zvva 252,102,152
s_waitcnt lgkmcnt(0)
E 10
Mvva 252,106,156
Ba 112,1,4096
C 94,8
Zvva 248,106,148
E 11
Mvva 248,102,144
Ba 116,1,4352
C 95,10
L2x24%=:
Yvaa 8,96,0,16
U 12
U 13
Mvaa 8,100,4
E 12
Mvaa 8,104,8
U 14
U 15
Mvaa 8,108,12
E 13
Yvaa 124,104,24,16
E 14
Mvaa 124,108,28
Ba 120,1,4608
C 108,12
Mvaa 124,96,16
E 15
Mvaa 124,100,20
s_add_u32 m0, s41, 24576
Ba 124,1,4864
C 109,14
v_smfmac_f32_16x16x64_bf16 a[160:163], a[184:187], v[38:45], v7
buffer_load_dwordx4 v4, s[56:59], s43 offen lds
Mvva 128,42,128
U 8
U 9
Mvva 128,38,132
E 8
Mvva 132,38,140
U 10
U 11
Mvva 132,42,136
buffer_load_dwordx4 v4, s[56:59], s43 offen offset:1024 lds
E 9
Mvva 140,56,136
s_xor_b32 s43, s43, 0x1e000
E 10
Mvva 140,52,140
Ta 146,2,512
C 32,8
Mvva 136,52,132
E 11
Mvva 136,56,128
Ta 144,2,2560
C 33,10
Yvaa 8,96,32,20
U 124
U 125
Mvaa 8,100,36
E 124
Mvaa 8,104,40
U 126
U 127
Mvaa 8,108,44
E 125
Yvaa 12,104,56,20
E 126
Mvaa 12,108,60
Ta 150,2,4608
C 46,124
Mvaa 12,96,48
E 127
Mvaa 12,100,52
s_add_u32 m0, s41, 57344
Ta 148,2,6656
C 47,126
v_smfmac_f32_16x16x64_bf16 a[164:167], a[184:187], v[52:59], v7
LPAD9%=:
s_cbranch_vccnz LRET%=
buffer_load_dwordx4 v28, s[60:63], s44 offen lds
Mvva 144,70,128
U 8
U 9
Mvva 144,66,132
E 8
Mvva 148,66,140
s_add_u32 m0, s41, 56320
U 10
U 11
Mvva 148,70,136
buffer_load_dwordx4 v28, s[60:63], s44 offen offset:2048 lds
E 9
Mava 196,84,136
s_xor_b32 s44, s44, 0x4080
E 10
Mava 196,80,140
Ta 158,3,4608
C 60,8
Mava 192,80,132
E 11
Mava 192,84,128
Ta 156,3,6656
C 61,10
Yvaa 8,96,64,24
U 12
U 13
Mvaa 8,100,68
buffer_store_dwordx4 v[116:119], v5, s[68:71], s46 offen sc1 nt
E 12
U 14
Mvaa 8,104,72
s_xor_b32 s46, s46, 0x7000
Ta 188,30,8192
U 15
Mvaa 8,108,76
s_add_u32 s46, s46, 0x8000
Ta 190,30,8256
E 13
Yvaa 124,104,88,24
E 14
Mvaa 124,108,92
Ta 154,3,512
C 74,12
Mvaa 124,96,80
E 15
Mvaa 124,100,84
Ta 152,3,2560
C 75,14
Mvva 152,98,128
s_add_u32 m0, s42, 7168
U 8
U 9
Mvva 152,94,132
buffer_load_dwordx4 v0, s[64:67], s45 offen lds
E 8
Mvva 156,94,140
s_xor_b32 s45, s45, 0x7000
U 10
U 11
Mvva 156,98,136
s_add_u32 s45, s45, 0x8000
E 9
Mava 204,112,136
s_waitcnt lgkmcnt(0)
E 10
Mava 204,108,140
Ba 96,1,8192
C 88,8
Mava 200,108,132
E 11
Mava 200,112,128
Ba 100,1,8448
C 89,10
Yvaa 8,112,0,16
U 124
U 125
Mvaa 8,116,4
E 124
Mvaa 8,120,8
U 126
U 127
Mvaa 8,124,12
E 125
Yvaa 12,120,24,16
E 126
Mvaa 12,124,28
Ba 104,1,8704
C 102,124
Mvaa 12,112,16
E 127
Mvaa 12,116,20
Ba 108,1,8960
C 103,126
Mvva 160,42,144
U 8
U 9
Mvva 160,38,148
E 8
Mvva 164,38,156
U 10
U 11
Mvva 164,42,152
E 9
Mava 212,56,152
E 10
Mava 212,52,156
Ta 130,2,8192
C 34,8
Mava 208,52,148
E 11
Mava 208,56,144
Ta 128,2,10240
C 35,10
Yvaa 8,112,32,20
U 12
U 13
Mvaa 8,116,36
E 12
Mvaa 8,120,40
U 14
U 15
Mvaa 8,124,44
E 13
Yvaa 124,120,56,20
E 14
Mvaa 124,124,60
Ta 134,2,12288
C 48,12
Mvaa 124,112,48
E 15
Mvaa 124,116,52
Ta 132,2,14336
C 49,14
Mvva 168,70,144
U 8
U 9
Mvva 168,66,148
buffer_store_dwordx4 a[188:191], v5, s[68:71], s46 offen sc1 nt
E 8
)ASM"
R"ASM(
U 10
Mvva 172,66,156
s_xor_b32 s46, s46, 0x1000
Tv 116,30,9216
U 11
Mvva 172,70,152
Tv 118,30,9280
E 9
Mava 220,84,152
E 10
Mava 220,80,156
Ta 142,3,12288
C 62,8
Mava 216,80,148
E 11
Mava 216,84,144
Ta 140,3,14336
C 63,10
Yvaa 8,112,64,24
U 124
U 125
Mvaa 8,116,68
E 124
Mvaa 8,120,72
U 126
U 127
Mvaa 8,124,76
E 125
Yvaa 12,120,88,24
E 126
Mvaa 12,124,92
Ta 138,3,8192
C 76,124
Mvaa 12,112,80
E 127
Mvaa 12,116,84
s_add_u32 m0, s42, 8192
Ta 136,3,10240
C 77,126
v_smfmac_f32_16x16x64_bf16 a[168:171], a[184:187], v[66:73], v7
buffer_load_dwordx4 v4, s[64:67], s45 offen lds
Mvva 176,98,144
s_xor_b32 s45, s45, 0x1000
U 8
U 9
Mvva 176,94,148
E 8
Mvva 180,94,156
U 10
U 11
Mvva 180,98,152
E 9
Mava 228,112,152
s_waitcnt lgkmcnt(0)
E 10
Mava 228,108,156
Ba 112,1,12288
C 90,8
Mava 224,108,148
E 11
Mava 224,112,144
Ba 116,1,12544
C 91,10
Yvaa 8,96,0,16
U 12
U 13
Mvaa 8,100,4
E 12
Mvaa 8,104,8
U 14
U 15
Mvaa 8,108,12
E 13
Yvaa 124,104,24,16
E 14
Mvaa 124,108,28
Ba 120,1,12800
C 104,12
Mvaa 124,96,16
E 15
Mvaa 124,100,20
Ba 124,1,13056
C 105,14
Mvva 184,42,128
U 8
U 9
Mvva 184,38,132
E 8
Mvva 188,38,140
U 10
U 11
Mvva 188,42,136
E 9
Mava 236,56,136
E 10
Mava 236,52,140
Ta 146,2,8704
C 36,8
Mava 232,52,132
E 11
Mava 232,56,128
Ta 144,2,10752
C 37,10
Yvaa 8,96,32,20
U 124
U 125
Mvaa 8,100,36
E 124
Mvaa 8,104,40
U 126
U 127
Mvaa 8,108,44
E 125
Yvaa 12,104,56,20
E 126
Mvaa 12,108,60
Ta 150,2,12800
C 50,124
Mvaa 12,96,48
E 127
Mvaa 12,100,52
Ta 148,2,14848
C 51,126
Mvva 192,70,128
U 8
U 9
Mvva 192,66,132
buffer_store_dwordx4 v[116:119], v5, s[68:71], s46 offen sc1 nt
E 8
U 10
Mvva 196,66,140
s_xor_b32 s46, s46, 0x3000
Ta 188,30,10240
U 11
Mvva 196,70,136
Ta 190,30,10304
E 9
Mava 244,84,136
E 10
Mava 244,80,140
Ta 158,3,12800
C 64,8
Mava 240,80,132
E 11
Mava 240,84,128
Ta 156,3,14848
C 65,10
Yvaa 8,96,64,24
U 12
U 13
Mvaa 8,100,68
E 12
Mvaa 8,104,72
U 14
U 15
Mvaa 8,108,76
E 13
Yvaa 124,104,88,24
E 14
Mvaa 124,108,92
Ta 154,3,8704
C 78,12
Mvaa 124,96,80
E 15
Mvaa 124,100,84
s_add_u32 m0, s42, 9216
Ta 152,3,10752
C 79,14
v_smfmac_f32_16x16x64_bf16 a[172:175], a[184:187], v[80:87], v7
buffer_load_dwordx4 v4, s[64:67], s45 offen lds
Mvva 200,98,128
s_xor_b32 s45, s45, 0x3000
U 8
U 9
Mvva 200,94,132
E 8
Mvva 204,94,140
U 10
U 11
Mvva 204,98,136
s_waitcnt vmcnt(6)
E 9
Mava 252,112,136
s_waitcnt lgkmcnt(0)
E 10
Mava 252,108,140
s_barrier
Ba 96,1,16384
C 92,8
Mava 248,108,132
E 11
Mava 248,112,128
Ba 100,1,16640
C 93,10
Yvaa 8,112,0,16
U 124
U 125
Mvaa 8,116,4
E 124
Mvaa 8,120,8
U 126
U 127
Mvaa 8,124,12
E 125
Yvaa 12,120,24,16
E 126
Mvaa 12,124,28
Ba 104,1,16896
C 106,124
Mvaa 12,112,16
E 127
Mvaa 12,116,20
s_add_u32 m0, s41, 0
Ba 108,1,17152
C 107,126
v_smfmac_f32_16x16x64_bf16 a[176:179], a[184:187], v[94:101], v7
buffer_load_dwordx4 v4, s[56:59], s43 offen lds
Mvva 208,42,144
U 8
U 9
Mvva 208,38,148
E 8
Mvva 212,38,156
U 10
U 11
Mvva 212,42,152
buffer_load_dwordx4 v4, s[56:59], s43 offen offset:1024 lds
E 9
Mvva 220,56,152
s_xor_b32 s43, s43, 0x2000
E 10
Mvva 220,52,156
Ta 128,2,16384
C 38,8
Mvva 216,52,148
E 11
Mvva 216,56,144
Ta 130,2,18432
C 39,10
Yvaa 8,112,32,20
U 12
U 13
Mvaa 8,116,36
E 12
Mvaa 8,120,40
U 14
U 15
Mvaa 8,124,44
E 13
Yvaa 124,120,56,20
E 14
Mvaa 124,124,60
Ta 132,2,20480
C 52,12
Mvaa 124,112,48
E 15
Mvaa 124,116,52
s_add_u32 m0, s41, 32768
Ta 134,2,22528
C 53,14
v_smfmac_f32_16x16x64_bf16 a[180:183], a[184:187], v[108:115], v7
buffer_load_dwordx4 v28, s[60:63], s44 offen lds
Mvva 224,70,144
U 8
U 9
Mvva 224,66,148
E 8
Mvva 228,66,156
s_add_u32 m0, s41, 31744
U 10
U 11
Mvva 228,70,152
buffer_load_dwordx4 v28, s[60:63], s44 offen offset:2048 lds
E 9
Mvva 236,84,152
s_xor_b32 s44, s44, 0x80
E 10
Mvva 236,80,156
Ta 140,3,20480
C 66,8
Mvva 232,80,148
E 11
Mvva 232,84,144
Ta 142,3,22528
C 67,10
Yvaa 8,112,64,24
U 124
U 125
Mvaa 8,116,68
E 124
Mvaa 8,120,72
U 126
U 127
Mvaa 8,124,76
E 125
Yvaa 12,120,88,24
E 126
Mvaa 12,124,92
Ta 136,3,16384
C 80,124
Mvaa 12,112,80
E 127
Mvaa 12,116,84
Ta 138,3,18432
C 81,126
v_add_u32 v120, L2x60%=-LPF%=, v122
Mvva 240,98,144
U 8
U 9
Mvva 240,94,148
buffer_load_dword v120, v120, s[72:75], 0 offen
E 8
Mvva 244,94,156
U 10
U 11
Mvva 244,98,152
E 9
Mvva 252,112,152
s_waitcnt lgkmcnt(0)
E 10
Mvva 252,108,156
Ba 112,1,20480
C 94,8
Mvva 248,108,148
E 11
Mvva 248,112,144
Ba 116,1,20736
C 95,10
L2x36%=:
Yvaa 8,96,0,16
U 12
U 13
Mvaa 8,100,4
E 12
Mvaa 8,104,8
U 14
U 15
Mvaa 8,108,12
E 13
Yvaa 124,104,24,16
E 14
Mvaa 124,108,28
Ba 120,1,20992
C 108,12
Mvaa 124,96,16
E 15
Mvaa 124,100,20
s_add_u32 m0, s41, 8192
Ba 124,1,21248
C 109,14
v_smfmac_f32_16x16x64_bf16 a[160:163], a[184:187], v[32:39], v7
buffer_load_dwordx4 v4, s[56:59], s43 offen lds
Mvva 128,32,128
U 8
U 9
Mvva 128,36,132
E 8
Mvva 132,36,140
U 10
U 11
Mvva 132,32,136
buffer_load_dwordx4 v4, s[56:59], s43 offen offset:1024 lds
E 9
Mvva 140,46,136
s_xor_b32 s43, s43, 0x6000
E 10
Mvva 140,50,140
Ta 144,2,16896
C 44,8
Mvva 136,50,132
E 11
Mvva 136,46,128
Ta 146,2,18944
C 45,10
Yvaa 8,96,32,20
U 124
U 125
Mvaa 8,100,36
E 124
Mvaa 8,104,40
U 126
U 127
Mvaa 8,108,44
E 125
Yvaa 12,104,56,20
E 126
Mvaa 12,108,60
Ta 148,2,20992
C 58,124
Mvaa 12,96,48
E 127
Mvaa 12,100,52
s_add_u32 m0, s41, 40960
Ta 150,2,23040
C 59,126
v_smfmac_f32_16x16x64_bf16 a[164:167], a[184:187], v[46:53], v7
buffer_load_dwordx4 v28, s[60:63], s44 offen lds
Mvva 144,60,128
U 8
U 9
)ASM"
R"ASM(
Mvva 144,64,132
E 8
Mvva 148,64,140
s_add_u32 m0, s41, 39936
U 10
U 11
Mvva 148,60,136
buffer_load_dwordx4 v28, s[60:63], s44 offen offset:2048 lds
E 9
Mava 196,74,136
s_xor_b32 s44, s44, 0x1c080
E 10
Mava 196,78,140
Ta 156,3,20992
C 72,8
Mava 192,78,132
E 11
Mava 192,74,128
Ta 158,3,23040
C 73,10
Yvaa 8,96,64,24
U 12
U 13
Mvaa 8,100,68
E 12
Mvaa 8,104,72
U 14
U 15
Mvaa 8,108,76
E 13
Yvaa 124,104,88,24
E 14
Mvaa 124,108,92
Ta 152,3,16896
C 86,12
Mvaa 124,96,80
E 15
Mvaa 124,100,84
Ta 154,3,18944
C 87,14
Mvva 152,88,128
U 8
U 9
Mvva 152,92,132
E 8
Mvva 156,92,140
U 10
U 11
Mvva 156,88,136
E 9
Mava 204,102,136
s_waitcnt lgkmcnt(0)
E 10
Mava 204,106,140
Ba 96,1,24576
C 100,8
Mava 200,106,132
E 11
Mava 200,102,128
Ba 100,1,24832
C 101,10
Yvaa 8,112,0,16
U 124
U 125
Mvaa 8,116,4
E 124
Mvaa 8,120,8
U 126
U 127
Mvaa 8,124,12
E 125
Yvaa 12,120,24,16
E 126
Mvaa 12,124,28
Ba 104,1,25088
C 114,124
Mvaa 12,112,16
E 127
Mvaa 12,116,20
Ba 108,1,25344
C 115,126
Mvva 160,32,144
U 8
U 9
Mvva 160,36,148
LPAD10%=:
s_cbranch_vccnz LRET%=
E 8
Mvva 164,36,156
U 10
U 11
Mvva 164,32,152
E 9
Mava 212,46,152
E 10
Mava 212,50,156
Ta 128,2,24576
C 42,8
Mava 208,50,148
E 11
Mava 208,46,144
Ta 130,2,26624
C 43,10
Yvaa 8,112,32,20
U 12
U 13
Mvaa 8,116,36
E 12
Mvaa 8,120,40
U 14
U 15
Mvaa 8,124,44
E 13
Yvaa 124,120,56,20
E 14
Mvaa 124,124,60
Ta 132,2,28672
C 56,12
Mvaa 124,112,48
E 15
Mvaa 124,116,52
Ta 134,2,30720
C 57,14
Mvva 168,60,144
U 8
U 9
Mvva 168,64,148
buffer_store_dwordx4 a[188:191], v5, s[68:71], s46 offen sc1 nt
E 8
U 10
Mvva 172,64,156
s_xor_b32 s46, s46, 0x1000
Tv 116,30,11264
U 11
Mvva 172,60,152
Tv 118,30,11328
E 9
Mava 220,74,152
E 10
Mava 220,78,156
Ta 140,3,28672
C 70,8
Mava 216,78,148
E 11
Mava 216,74,144
Ta 142,3,30720
C 71,10
Yvaa 8,112,64,24
U 124
U 125
Mvaa 8,116,68
E 124
Mvaa 8,120,72
U 126
U 127
Mvaa 8,124,76
E 125
Yvaa 12,120,88,24
E 126
Mvaa 12,124,92
Ta 136,3,24576
C 84,124
Mvaa 12,112,80
E 127
Mvaa 12,116,84
s_add_u32 m0, s42, 10240
Ta 138,3,26624
C 85,126
v_smfmac_f32_16x16x64_bf16 a[168:171], a[184:187], v[60:67], v7
buffer_load_dwordx4 v0, s[64:67], s45 offen lds
Mvva 176,88,144
s_xor_b32 s45, s45, 0x1000
U 8
U 9
Mvva 176,92,148
E 8
Mvva 180,92,156
U 10
U 11
Mvva 180,88,152
E 9
Mava 228,102,152
s_waitcnt lgkmcnt(0)
E 10
Mava 228,106,156
Ba 112,1,28672
C 98,8
Mava 224,106,148
E 11
Mava 224,102,144
Ba 116,1,28928
C 99,10
Yvaa 8,96,0,16
U 12
U 13
Mvaa 8,100,4
E 12
Mvaa 8,104,8
U 14
U 15
Mvaa 8,108,12
E 13
Yvaa 124,104,24,16
E 14
Mvaa 124,108,28
Ba 120,1,29184
C 112,12
Mvaa 124,96,16
E 15
Mvaa 124,100,20
Ba 124,1,29440
C 113,14
Mvva 184,32,128
U 8
U 9
Mvva 184,36,132
E 8
Mvva 188,36,140
U 10
U 11
Mvva 188,32,136
E 9
Mava 236,46,136
E 10
Mava 236,50,140
Ta 144,2,25088
C 40,8
Mava 232,50,132
E 11
Mava 232,46,128
Ta 146,2,27136
C 41,10
Yvaa 8,96,32,20
U 124
U 125
Mvaa 8,100,36
E 124
Mvaa 8,104,40
U 126
U 127
Mvaa 8,108,44
E 125
Yvaa 12,104,56,20
E 126
Mvaa 12,108,60
Ta 148,2,29184
C 54,124
Mvaa 12,96,48
E 127
Mvaa 12,100,52
Ta 150,2,31232
C 55,126
Mvva 192,60,128
U 8
U 9
Mvva 192,64,132
buffer_store_dwordx4 v[116:119], v5, s[68:71], s46 offen sc1 nt
E 8
U 10
Mvva 196,64,140
s_xor_b32 s46, s46, 0x7000
Ta 188,30,12288
U 11
Mvva 196,60,136
Ta 190,30,12352
E 9
Mava 244,74,136
E 10
Mava 244,78,140
Ta 156,3,29184
C 68,8
Mava 240,78,132
E 11
Mava 240,74,128
Ta 158,3,31232
C 69,10
Yvaa 8,96,64,24
U 12
U 13
Mvaa 8,100,68
E 12
Mvaa 8,104,72
U 14
U 15
Mvaa 8,108,76
E 13
Yvaa 124,104,88,24
E 14
Mvaa 124,108,92
Ta 152,3,25088
C 82,12
Mvaa 124,96,80
E 15
Mvaa 124,100,84
s_add_u32 m0, s42, 11264
Ta 154,3,27136
C 83,14
v_smfmac_f32_16x16x64_bf16 a[172:175], a[184:187], v[74:81], v7
buffer_load_dwordx4 v0, s[64:67], s45 offen lds
Mvva 200,88,128
s_xor_b32 s45, s45, 0x7000
U 8
U 9
Mvva 200,92,132
E 8
Mvva 204,92,140
U 10
U 11
Mvva 204,88,136
s_waitcnt vmcnt(4)
E 9
Mava 252,102,136
s_waitcnt lgkmcnt(0)
E 10
Mava 252,106,140
s_barrier
Ba 96,1,0
C 96,8
Mava 248,106,132
E 11
Mava 248,102,128
Ba 100,1,256
C 97,10
Yvaa 8,112,0,16
U 124
U 125
Mvaa 8,116,4
E 124
Mvaa 8,120,8
U 126
U 127
Mvaa 8,124,12
E 125
Yvaa 12,120,24,16
E 126
Mvaa 12,124,28
Ba 104,1,512
C 110,124
Mvaa 12,112,16
E 127
Mvaa 12,116,20
s_add_u32 m0, s41, 16384
Ba 108,1,768
C 111,126
v_smfmac_f32_16x16x64_bf16 a[176:179], a[184:187], v[88:95], v7
buffer_load_dwordx4 v4, s[56:59], s43 offen lds
Mvva 208,32,144
U 8
U 9
Mvva 208,36,148
E 8
Mvva 212,36,156
U 10
U 11
Mvva 212,32,152
buffer_load_dwordx4 v4, s[56:59], s43 offen offset:1024 lds
E 9
Mvva 220,46,152
s_xor_b32 s43, s43, 0x2000
E 10
Mvva 220,50,156
Ta 130,2,0
C 38,8
Mvva 216,50,148
E 11
Mvva 216,46,144
Ta 128,2,2048
C 39,10
Yvaa 8,112,32,20
U 12
U 13
Mvaa 8,116,36
E 12
Mvaa 8,120,40
U 14
U 15
Mvaa 8,124,44
E 13
Yvaa 124,120,56,20
E 14
Mvaa 124,124,60
Ta 134,2,4096
C 52,12
Mvaa 124,112,48
E 15
Mvaa 124,116,52
s_add_u32 m0, s41, 49152
Ta 132,2,6144
C 53,14
v_smfmac_f32_16x16x64_bf16 a[180:183], a[184:187], v[102:109], v7
buffer_load_dwordx4 v28, s[60:63], s44 offen lds
Mvva 224,60,144
U 8
U 9
Mvva 224,64,148
E 8
Mvva 228,64,156
s_add_u32 m0, s41, 48128
U 10
U 11
)ASM"
R"ASM(
Mvva 228,60,152
buffer_load_dwordx4 v28, s[60:63], s44 offen offset:2048 lds
E 9
Mvva 236,74,152
s_xor_b32 s44, s44, 0x80
E 10
Mvva 236,78,156
Ta 142,3,4096
C 66,8
Mvva 232,78,148
E 11
Mvva 232,74,144
Ta 140,3,6144
C 67,10
Yvaa 8,112,64,24
U 124
U 125
Mvaa 8,116,68
E 124
Mvaa 8,120,72
U 126
U 127
Mvaa 8,124,76
E 125
Yvaa 12,120,88,24
E 126
Mvaa 12,124,92
Ta 138,3,0
C 80,124
Mvaa 12,112,80
E 127
Mvaa 12,116,84
Ta 136,3,2048
C 81,126
Mvva 240,88,144
U 8
U 9
Mvva 240,92,148
E 8
Mvva 244,92,156
U 10
U 11
Mvva 244,88,152
E 9
Mvva 252,102,152
s_waitcnt lgkmcnt(0)
E 10
Mvva 252,106,156
Ba 112,1,4096
C 94,8
Mvva 248,106,148
E 11
Mvva 248,102,144
Ba 116,1,4352
C 95,10
L2x48%=:
Yvaa 8,96,0,16
U 12
U 13
Mvaa 8,100,4
E 12
Mvaa 8,104,8
U 14
U 15
Mvaa 8,108,12
E 13
Yvaa 124,104,24,16
E 14
Mvaa 124,108,28
Ba 120,1,4608
C 108,12
Mvaa 124,96,16
E 15
Mvaa 124,100,20
s_add_u32 m0, s41, 24576
Ba 124,1,4864
C 109,14
v_smfmac_f32_16x16x64_bf16 a[160:163], a[184:187], v[38:45], v7
buffer_load_dwordx4 v4, s[56:59], s43 offen lds
Mvva 128,42,128
U 8
U 9
Mvva 128,38,132
E 8
Mvva 132,38,140
U 10
U 11
Mvva 132,42,136
buffer_load_dwordx4 v4, s[56:59], s43 offen offset:1024 lds
E 9
Mvva 140,56,136
s_xor_b32 s43, s43, 0xe000
E 10
Mvva 140,52,140
Ta 146,2,512
C 32,8
Mvva 136,52,132
E 11
Mvva 136,56,128
Ta 144,2,2560
C 33,10
Yvaa 8,96,32,20
U 124
U 125
Mvaa 8,100,36
E 124
Mvaa 8,104,40
U 126
U 127
Mvaa 8,108,44
E 125
Yvaa 12,104,56,20
E 126
Mvaa 12,108,60
Ta 150,2,4608
C 46,124
Mvaa 12,96,48
E 127
Mvaa 12,100,52
s_add_u32 m0, s41, 57344
Ta 148,2,6656
C 47,126
v_smfmac_f32_16x16x64_bf16 a[164:167], a[184:187], v[52:59], v7
buffer_load_dwordx4 v28, s[60:63], s44 offen lds
Mvva 144,70,128
U 8
U 9
Mvva 144,66,132
E 8
Mvva 148,66,140
s_add_u32 m0, s41, 56320
U 10
U 11
Mvva 148,70,136
buffer_load_dwordx4 v28, s[60:63], s44 offen offset:2048 lds
E 9
Mava 196,84,136
s_xor_b32 s44, s44, 0x4080
E 10
Mava 196,80,140
Ta 158,3,4608
C 60,8
Mava 192,80,132
E 11
Mava 192,84,128
Ta 156,3,6656
C 61,10
Yvaa 8,96,64,24
U 12
U 13
Mvaa 8,100,68
buffer_store_dwordx4 a[188:191], v5, s[68:71], s46 offen sc1 nt
E 12
U 14
Mvaa 8,104,72
s_xor_b32 s46, s46, 0x1000
Tv 116,30,13312
U 15
Mvaa 8,108,76
Tv 118,30,13376
E 13
Yvaa 124,104,88,24
E 14
Mvaa 124,108,92
Ta 154,3,512
C 74,12
Mvaa 124,96,80
E 15
Mvaa 124,100,84
Ta 152,3,2560
C 75,14
Mvva 152,98,128
s_add_u32 m0, s42, 12288
U 8
U 9
Mvva 152,94,132
buffer_load_dwordx4 v4, s[64:67], s45 offen lds
E 8
Mvva 156,94,140
s_xor_b32 s45, s45, 0x1000
U 10
U 11
Mvva 156,98,136
E 9
Mava 204,112,136
s_waitcnt lgkmcnt(0)
E 10
Mava 204,108,140
Ba 96,1,8192
C 88,8
Mava 200,108,132
E 11
Mava 200,112,128
Ba 100,1,8448
C 89,10
Yvaa 8,112,0,16
U 124
U 125
Mvaa 8,116,4
E 124
Mvaa 8,120,8
U 126
U 127
Mvaa 8,124,12
E 125
Yvaa 12,120,24,16
E 126
Mvaa 12,124,28
Ba 104,1,8704
C 102,124
Mvaa 12,112,16
E 127
Mvaa 12,116,20
Ba 108,1,8960
C 103,126
Mvva 160,42,144
U 8
U 9
Mvva 160,38,148
E 8
Mvva 164,38,156
U 10
U 11
Mvva 164,42,152
E 9
Mava 212,56,152
E 10
Mava 212,52,156
Ta 130,2,8192
C 34,8
Mava 208,52,148
E 11
Mava 208,56,144
Ta 128,2,10240
C 35,10
Yvaa 8,112,32,20
U 12
U 13
Mvaa 8,116,36
E 12
Mvaa 8,120,40
U 14
U 15
Mvaa 8,124,44
E 13
Yvaa 124,120,56,20
E 14
Mvaa 124,124,60
Ta 134,2,12288
C 48,12
Mvaa 124,112,48
E 15
Mvaa 124,116,52
Ta 132,2,14336
C 49,14
Mvva 168,70,144
U 8
U 9
Mvva 168,66,148
buffer_store_dwordx4 v[116:119], v5, s[68:71], s46 offen sc1 nt
E 8
U 10
Mvva 172,66,156
s_xor_b32 s46, s46, 0x3000
Ta 188,30,14336
U 11
Mvva 172,70,152
Ta 190,30,14400
E 9
Mava 220,84,152
E 10
Mava 220,80,156
Ta 142,3,12288
C 62,8
Mava 216,80,148
E 11
Mava 216,84,144
Ta 140,3,14336
C 63,10
Yvaa 8,112,64,24
U 124
U 125
Mvaa 8,116,68
E 124
Mvaa 8,120,72
U 126
U 127
Mvaa 8,124,76
E 125
Yvaa 12,120,88,24
E 126
Mvaa 12,124,92
Ta 138,3,8192
C 76,124
Mvaa 12,112,80
E 127
Mvaa 12,116,84
s_add_u32 m0, s42, 13312
Ta 136,3,10240
C 77,126
v_smfmac_f32_16x16x64_bf16 a[168:171], a[184:187], v[66:73], v7
buffer_load_dwordx4 v4, s[64:67], s45 offen lds
Mvva 176,98,144
s_xor_b32 s45, s45, 0x3000
U 8
U 9
Mvva 176,94,148
E 8
Mvva 180,94,156
U 10
U 11
Mvva 180,98,152
E 9
Mava 228,112,152
s_waitcnt lgkmcnt(0)
E 10
Mava 228,108,156
LPAD11%=:
s_cbranch_vccnz LRET%=
Ba 112,1,12288
C 90,8
Mava 224,108,148
E 11
Mava 224,112,144
Ba 116,1,12544
C 91,10
Yvaa 8,96,0,16
U 12
U 13
Mvaa 8,100,4
E 12
Mvaa 8,104,8
U 14
U 15
Mvaa 8,108,12
E 13
Yvaa 124,104,24,16
E 14
Mvaa 124,108,28
Ba 120,1,12800
C 104,12
Mvaa 124,96,16
E 15
Mvaa 124,100,20
Ba 124,1,13056
C 105,14
Mvva 184,42,128
U 8
U 9
Mvva 184,38,132
E 8
Mvva 188,38,140
U 10
U 11
Mvva 188,42,136
E 9
Mava 236,56,136
E 10
Mava 236,52,140
Ta 146,2,8704
C 36,8
Mava 232,52,132
E 11
Mava 232,56,128
Ta 144,2,10752
C 37,10
Yvaa 8,96,32,20
U 124
U 125
Mvaa 8,100,36
E 124
Mvaa 8,104,40
U 126
U 127
Mvaa 8,108,44
E 125
Yvaa 12,104,56,20
E 126
Mvaa 12,108,60
Ta 150,2,12800
C 50,124
Mvaa 12,96,48
E 127
Mvaa 12,100,52
Ta 148,2,14848
C 51,126
Mvva 192,70,128
U 8
U 9
Mvva 192,66,132
buffer_store_dwordx4 a[188:191], v5, s[68:71], s46 offen sc1 nt
E 8
U 10
Mvva 196,66,140
s_xor_b32 s46, s46, 0x1000
Tv 116,30,15360
U 11
Mvva 196,70,136
Tv 118,30,15424
E 9
)ASM"
R"ASM(
Mava 244,84,136
E 10
Mava 244,80,140
Ta 158,3,12800
C 64,8
Mava 240,80,132
E 11
Mava 240,84,128
Ta 156,3,14848
C 65,10
Yvaa 8,96,64,24
U 12
U 13
Mvaa 8,100,68
E 12
Mvaa 8,104,72
U 14
U 15
Mvaa 8,108,76
E 13
Yvaa 124,104,88,24
E 14
Mvaa 124,108,92
Ta 154,3,8704
C 78,12
Mvaa 124,96,80
E 15
Mvaa 124,100,84
s_add_u32 m0, s42, 14336
Ta 152,3,10752
C 79,14
v_smfmac_f32_16x16x64_bf16 a[172:175], a[184:187], v[80:87], v7
buffer_load_dwordx4 v0, s[64:67], s45 offen lds
Mvva 200,98,128
s_xor_b32 s45, s45, 0x1000
U 8
U 9
Mvva 200,94,132
E 8
Mvva 204,94,140
U 10
U 11
Mvva 204,98,136
s_waitcnt vmcnt(6)
E 9
Mava 252,112,136
s_waitcnt lgkmcnt(0)
E 10
Mava 252,108,140
s_barrier
Ba 96,1,16384
C 92,8
Mava 248,108,132
E 11
Mava 248,112,128
Ba 100,1,16640
C 93,10
Yvaa 8,112,0,16
U 124
U 125
Mvaa 8,116,4
E 124
Mvaa 8,120,8
U 126
U 127
Mvaa 8,124,12
E 125
Yvaa 12,120,24,16
E 126
Mvaa 12,124,28
Ba 104,1,16896
C 106,124
Mvaa 12,112,16
E 127
Mvaa 12,116,20
s_add_u32 m0, s41, 0
Ba 108,1,17152
C 107,126
v_smfmac_f32_16x16x64_bf16 a[176:179], a[184:187], v[94:101], v7
buffer_load_dwordx4 v4, s[56:59], s43 offen lds
Mvva 208,42,144
U 8
U 9
Mvva 208,38,148
E 8
Mvva 212,38,156
U 10
U 11
Mvva 212,42,152
buffer_load_dwordx4 v4, s[56:59], s43 offen offset:1024 lds
E 9
Mvva 220,56,152
s_xor_b32 s43, s43, 0x2000
E 10
Mvva 220,52,156
Ta 128,2,16384
C 38,8
Mvva 216,52,148
E 11
Mvva 216,56,144
Ta 130,2,18432
C 39,10
Yvaa 8,112,32,20
U 12
U 13
Mvaa 8,116,36
E 12
Mvaa 8,120,40
U 14
U 15
Mvaa 8,124,44
E 13
Yvaa 124,120,56,20
E 14
Mvaa 124,124,60
Ta 132,2,20480
C 52,12
Mvaa 124,112,48
E 15
Mvaa 124,116,52
s_add_u32 m0, s41, 32768
Ta 134,2,22528
C 53,14
v_smfmac_f32_16x16x64_bf16 a[180:183], a[184:187], v[108:115], v7
buffer_load_dwordx4 v28, s[60:63], s44 offen lds
Mvva 224,70,144
U 8
U 9
Mvva 224,66,148
E 8
Mvva 228,66,156
s_add_u32 m0, s41, 31744
U 10
U 11
Mvva 228,70,152
buffer_load_dwordx4 v28, s[60:63], s44 offen offset:2048 lds
E 9
Mvva 236,84,152
s_xor_b32 s44, s44, 0x80
E 10
Mvva 236,80,156
Ta 140,3,20480
C 66,8
Mvva 232,80,148
E 11
Mvva 232,84,144
Ta 142,3,22528
C 67,10
Yvaa 8,112,64,24
U 124
U 125
Mvaa 8,116,68
E 124
Mvaa 8,120,72
U 126
U 127
Mvaa 8,124,76
E 125
Yvaa 12,120,88,24
E 126
Mvaa 12,124,92
Ta 136,3,16384
C 80,124
Mvaa 12,112,80
E 127
Mvaa 12,116,84
Ta 138,3,18432
C 81,126
v_add_u32 v120, L2x84%=-LPF%=, v122
Mvva 240,98,144
U 8
U 9
Mvva 240,94,148
buffer_load_dword v120, v120, s[72:75], 0 offen
E 8
Mvva 244,94,156
U 10
U 11
Mvva 244,98,152
E 9
Mvva 252,112,152
s_waitcnt lgkmcnt(0)
E 10
Mvva 252,108,156
Ba 112,1,20480
C 94,8
Mvva 248,108,148
E 11
Mvva 248,112,144
Ba 116,1,20736
C 95,10
L2x60%=:
Yvaa 8,96,0,16
U 12
U 13
Mvaa 8,100,4
E 12
Mvaa 8,104,8
U 14
U 15
Mvaa 8,108,12
E 13
Yvaa 124,104,24,16
E 14
Mvaa 124,108,28
Ba 120,1,20992
C 108,12
Mvaa 124,96,16
E 15
Mvaa 124,100,20
s_add_u32 m0, s41, 8192
Ba 124,1,21248
C 109,14
v_smfmac_f32_16x16x64_bf16 a[160:163], a[184:187], v[32:39], v7
buffer_load_dwordx4 v4, s[56:59], s43 offen lds
Mvva 128,32,128
U 8
U 9
Mvva 128,36,132
E 8
Mvva 132,36,140
U 10
U 11
Mvva 132,32,136
buffer_load_dwordx4 v4, s[56:59], s43 offen offset:1024 lds
E 9
Mvva 140,46,136
s_xor_b32 s43, s43, 0x6000
E 10
Mvva 140,50,140
Ta 144,2,16896
C 44,8
Mvva 136,50,132
E 11
Mvva 136,46,128
Ta 146,2,18944
C 45,10
Yvaa 8,96,32,20
U 124
U 125
Mvaa 8,100,36
E 124
Mvaa 8,104,40
U 126
U 127
Mvaa 8,108,44
E 125
Yvaa 12,104,56,20
E 126
Mvaa 12,108,60
Ta 148,2,20992
C 58,124
Mvaa 12,96,48
E 127
Mvaa 12,100,52
s_add_u32 m0, s41, 40960
Ta 150,2,23040
C 59,126
v_smfmac_f32_16x16x64_bf16 a[164:167], a[184:187], v[46:53], v7
buffer_load_dwordx4 v28, s[60:63], s44 offen lds
Mvva 144,60,128
U 8
U 9
Mvva 144,64,132
E 8
Mvva 148,64,140
s_add_u32 m0, s41, 39936
U 10
U 11
Mvva 148,60,136
buffer_load_dwordx4 v28, s[60:63], s44 offen offset:2048 lds
E 9
Mava 196,74,136
s_xor_b32 s44, s44, 0xc080
E 10
Mava 196,78,140
Ta 156,3,20992
C 72,8
Mava 192,78,132
E 11
Mava 192,74,128
Ta 158,3,23040
C 73,10
Yvaa 8,96,64,24
U 12
U 13
Mvaa 8,100,68
E 12
Mvaa 8,104,72
U 14
U 15
Mvaa 8,108,76
E 13
Yvaa 124,104,88,24
E 14
Mvaa 124,108,92
Ta 152,3,16896
C 86,12
Mvaa 124,96,80
E 15
Mvaa 124,100,84
Ta 154,3,18944
C 87,14
Mvva 152,88,128
U 8
U 9
Mvva 152,92,132
E 8
Mvva 156,92,140
U 10
U 11
Mvva 156,88,136
E 9
Mava 204,102,136
s_waitcnt lgkmcnt(0)
E 10
Mava 204,106,140
Ba 96,1,24576
C 100,8
Mava 200,106,132
E 11
Mava 200,102,128
Ba 100,1,24832
C 101,10
Yvaa 8,112,0,16
U 124
U 125
Mvaa 8,116,4
E 124
Mvaa 8,120,8
U 126
U 127
Mvaa 8,124,12
E 125
Yvaa 12,120,24,16
E 126
Mvaa 12,124,28
Ba 104,1,25088
C 114,124
Mvaa 12,112,16
E 127
Mvaa 12,116,20
Ba 108,1,25344
C 115,126
Mvva 160,32,144
U 8
U 9
Mvva 160,36,148
E 8
Mvva 164,36,156
U 10
U 11
Mvva 164,32,152
E 9
Mava 212,46,152
E 10
Mava 212,50,156
Ta 128,2,24576
C 42,8
Mava 208,50,148
E 11
Mava 208,46,144
Ta 130,2,26624
C 43,10
Yvaa 8,112,32,20
U 12
U 13
Mvaa 8,116,36
E 12
Mvaa 8,120,40
U 14
U 15
Mvaa 8,124,44
E 13
Yvaa 124,120,56,20
E 14
Mvaa 124,124,60
Ta 132,2,28672
C 56,12
Mvaa 124,112,48
E 15
Mvaa 124,116,52
Ta 134,2,30720
C 57,14
Mvva 168,60,144
U 8
U 9
Mvva 168,64,148
buffer_store_dwordx4 v[116:119], v5, s[68:71], s46 offen sc1 nt
E 8
U 10
Mvva 172,64,156
s_xor_b32 s46, s46, 0x7000
Ta 188,30,16384
U 11
Mvva 172,60,152
s_add_u32 s46, s46, 0x8000
Ta 190,30,16448
E 9
Mava 220,74,152
)ASM"
R"ASM(
E 10
Mava 220,78,156
Ta 140,3,28672
C 70,8
Mava 216,78,148
E 11
Mava 216,74,144
Ta 142,3,30720
C 71,10
Yvaa 8,112,64,24
U 124
U 125
Mvaa 8,116,68
E 124
Mvaa 8,120,72
U 126
U 127
Mvaa 8,124,76
E 125
Yvaa 12,120,88,24
E 126
Mvaa 12,124,92
Ta 136,3,24576
C 84,124
Mvaa 12,112,80
E 127
Mvaa 12,116,84
s_add_u32 m0, s42, 15360
Ta 138,3,26624
C 85,126
v_smfmac_f32_16x16x64_bf16 a[168:171], a[184:187], v[60:67], v7
buffer_load_dwordx4 v0, s[64:67], s45 offen lds
Mvva 176,88,144
s_xor_b32 s45, s45, 0x7000
U 8
U 9
Mvva 176,92,148
s_add_u32 s45, s45, 0x8000
E 8
Mvva 180,92,156
U 10
U 11
Mvva 180,88,152
E 9
Mava 228,102,152
s_waitcnt lgkmcnt(0)
E 10
Mava 228,106,156
Ba 112,1,28672
C 98,8
Mava 224,106,148
E 11
Mava 224,102,144
Ba 116,1,28928
C 99,10
Yvaa 8,96,0,16
U 12
U 13
Mvaa 8,100,4
E 12
Mvaa 8,104,8
U 14
U 15
Mvaa 8,108,12
E 13
Yvaa 124,104,24,16
E 14
Mvaa 124,108,28
Ba 120,1,29184
C 112,12
Mvaa 124,96,16
E 15
Mvaa 124,100,20
Ba 124,1,29440
C 113,14
Mvva 184,32,128
U 8
U 9
Mvva 184,36,132
E 8
Mvva 188,36,140
U 10
U 11
Mvva 188,32,136
E 9
Mava 236,46,136
E 10
Mava 236,50,140
Ta 144,2,25088
C 40,8
Mava 232,50,132
E 11
Mava 232,46,128
Ta 146,2,27136
C 41,10
Yvaa 8,96,32,20
U 124
U 125
Mvaa 8,100,36
E 124
Mvaa 8,104,40
U 126
U 127
Mvaa 8,108,44
E 125
Yvaa 12,104,56,20
E 126
Mvaa 12,108,60
Ta 148,2,29184
C 54,124
Mvaa 12,96,48
E 127
Mvaa 12,100,52
Ta 150,2,31232
C 55,126
Mvva 192,60,128
U 8
U 9
Mvva 192,64,132
buffer_store_dwordx4 a[188:191], v5, s[68:71], s46 offen sc1 nt
E 8
U 10
Mvva 196,64,140
s_xor_b32 s46, s46, 0x1000
Tv 116,30,17408
U 11
Mvva 196,60,136
Tv 118,30,17472
E 9
Mava 244,74,136
E 10
Mava 244,78,140
Ta 156,3,29184
C 68,8
Mava 240,78,132
E 11
Mava 240,74,128
Ta 158,3,31232
C 69,10
Yvaa 8,96,64,24
U 12
U 13
Mvaa 8,100,68
E 12
Mvaa 8,104,72
U 14
U 15
Mvaa 8,108,76
E 13
Yvaa 124,104,88,24
E 14
Mvaa 124,108,92
LPAD12%=:
s_cbranch_vccnz LRET%=
Ta 152,3,25088
C 82,12
Mvaa 124,96,80
E 15
Mvaa 124,100,84
s_add_u32 m0, s42, 16384
Ta 154,3,27136
C 83,14
v_smfmac_f32_16x16x64_bf16 a[172:175], a[184:187], v[74:81], v7
buffer_load_dwordx4 v4, s[64:67], s45 offen lds
Mvva 200,88,128
s_xor_b32 s45, s45, 0x1000
U 8
U 9
Mvva 200,92,132
E 8
Mvva 204,92,140
U 10
U 11
Mvva 204,88,136
s_waitcnt vmcnt(9)
E 9
Mava 252,102,136
s_waitcnt lgkmcnt(0)
E 10
Mava 252,106,140
s_barrier
Ba 96,1,0
C 96,8
Mava 248,106,132
E 11
Mava 248,102,128
Ba 100,1,256
C 97,10
Yvaa 8,112,0,16
U 124
U 125
Mvaa 8,116,4
E 124
Mvaa 8,120,8
U 126
U 127
Mvaa 8,124,12
E 125
Yvaa 12,120,24,16
E 126
Mvaa 12,124,28
Ba 104,1,512
C 110,124
Mvaa 12,112,16
E 127
Mvaa 12,116,20
s_add_u32 m0, s41, 16384
Ba 108,1,768
C 111,126
v_smfmac_f32_16x16x64_bf16 a[176:179], a[184:187], v[88:95], v7
buffer_load_dwordx4 v4, s[56:59], s43 offen lds
Mvva 208,32,144
U 8
U 9
Mvva 208,36,148
E 8
Mvva 212,36,156
U 10
U 11
Mvva 212,32,152
buffer_load_dwordx4 v4, s[56:59], s43 offen offset:1024 lds
E 9
Mvva 220,46,152
s_xor_b32 s43, s43, 0x2000
E 10
Mvva 220,50,156
Ta 130,2,0
C 38,8
Mvva 216,50,148
E 11
Mvva 216,46,144
Ta 128,2,2048
C 39,10
Yvaa 8,112,32,20
U 12
U 13
Mvaa 8,116,36
E 12
Mvaa 8,120,40
U 14
U 15
Mvaa 8,124,44
E 13
Yvaa 124,120,56,20
E 14
Mvaa 124,124,60
Ta 134,2,4096
C 52,12
Mvaa 124,112,48
E 15
Mvaa 124,116,52
s_add_u32 m0, s41, 49152
Ta 132,2,6144
C 53,14
v_smfmac_f32_16x16x64_bf16 a[180:183], a[184:187], v[102:109], v7
buffer_load_dwordx4 v28, s[60:63], s44 offen lds
Mvva 224,60,144
U 8
U 9
Mvva 224,64,148
E 8
Mvva 228,64,156
s_add_u32 m0, s41, 48128
U 10
U 11
Mvva 228,60,152
buffer_load_dwordx4 v28, s[60:63], s44 offen offset:2048 lds
E 9
Mvva 236,74,152
s_xor_b32 s44, s44, 0x80
E 10
Mvva 236,78,156
Ta 142,3,4096
C 66,8
Mvva 232,78,148
E 11
Mvva 232,74,144
Ta 140,3,6144
C 67,10
Yvaa 8,112,64,24
U 124
U 125
Mvaa 8,116,68
buffer_store_dwordx4 v[116:119], v5, s[68:71], s46 offen sc1 nt
E 124
U 126
Mvaa 8,120,72
s_xor_b32 s46, s46, 0x3000
Ta 188,30,18432
U 127
Mvaa 8,124,76
Ta 190,30,18496
E 125
Yvaa 12,120,88,24
E 126
Mvaa 12,124,92
Ta 138,3,0
C 80,124
Mvaa 12,112,80
E 127
Mvaa 12,116,84
Ta 136,3,2048
C 81,126
v_add_u32 v120, LFIN%=-LPF%=, v122
Mvva 240,88,144
s_add_u32 m0, s42, 17408
U 8
U 9
Mvva 240,92,148
buffer_load_dword v120, v120, s[72:75], 0 offen
buffer_load_dwordx4 v4, s[64:67], s45 offen lds
E 8
Mvva 244,92,156
s_xor_b32 s45, s45, 0x3000
U 10
U 11
Mvva 244,88,152
E 9
Mvva 252,102,152
s_waitcnt lgkmcnt(0)
E 10
Mvva 252,106,156
Ba 112,1,4096
C 94,8
Mvva 248,106,148
E 11
Mvva 248,102,144
Ba 116,1,4352
C 95,10
L2x72%=:
Yvaa 8,96,0,16
U 12
U 13
Mvaa 8,100,4
E 12
Mvaa 8,104,8
U 14
U 15
Mvaa 8,108,12
E 13
Yvaa 124,104,24,16
E 14
Mvaa 124,108,28
Ba 120,1,4608
C 108,12
Mvaa 124,96,16
E 15
Mvaa 124,100,20
s_add_u32 m0, s41, 24576
Ba 124,1,4864
C 109,14
v_smfmac_f32_16x16x64_bf16 a[160:163], a[184:187], v[38:45], v7
buffer_load_dwordx4 v4, s[56:59], s43 offen lds
Mvva 128,42,128
U 8
U 9
Mvva 128,38,132
E 8
Mvva 132,38,140
U 10
U 11
Mvva 132,42,136
buffer_load_dwordx4 v4, s[56:59], s43 offen offset:1024 lds
E 9
Mvva 140,56,136
s_xor_b32 s43, s43, 0x1e000
E 10
Mvva 140,52,140
s_add_u32 s43, s43, 0x200000
Ta 146,2,512
C 32,8
Mvva 136,52,132
s_cmp_eq_u32 s47, 3
E 11
Mvva 136,56,128
s_cselect_b32 s58, 0, s58
Ta 144,2,2560
C 33,10
Yvaa 8,96,32,20
U 124
U 125
Mvaa 8,100,36
E 124
Mvaa 8,104,40
U 126
U 127
Mvaa 8,108,44
E 125
Yvaa 12,104,56,20
E 126
Mvaa 12,108,60
Ta 150,2,4608
C 46,124
Mvaa 12,96,48
E 127
Mvaa 12,100,52
s_add_u32 m0, s41, 57344
Ta 148,2,6656
C 47,126
v_smfmac_f32_16x16x64_bf16 a[164:167], a[184:187], v[52:59], v7
)ASM"
R"ASM(
buffer_load_dwordx4 v28, s[60:63], s44 offen lds
Mvva 144,70,128
U 8
U 9
Mvva 144,66,132
E 8
Mvva 148,66,140
s_add_u32 m0, s41, 56320
U 10
U 11
Mvva 148,70,136
buffer_load_dwordx4 v28, s[60:63], s44 offen offset:2048 lds
E 9
Mava 196,84,136
s_xor_b32 s44, s44, 0x4080
E 10
Mava 196,80,140
Ta 158,3,4608
C 60,8
Mava 192,80,132
E 11
Mava 192,84,128
Ta 156,3,6656
C 61,10
Yvaa 8,96,64,24
U 12
U 13
Mvaa 8,100,68
E 12
Mvaa 8,104,72
U 14
U 15
Mvaa 8,108,76
E 13
Yvaa 124,104,88,24
E 14
Mvaa 124,108,92
Ta 154,3,512
C 74,12
Mvaa 124,96,80
E 15
Mvaa 124,100,84
Ta 152,3,2560
C 75,14
Mvva 152,98,128
U 8
U 9
Mvva 152,94,132
E 8
Mvva 156,94,140
U 10
U 11
Mvva 156,98,136
s_waitcnt vmcnt(15)
E 9
Mava 204,112,136
s_waitcnt lgkmcnt(0)
E 10
Mava 204,108,140
s_barrier
Ba 96,1,8192
C 88,8
Mava 200,108,132
E 11
Mava 200,112,128
Ba 100,1,8448
C 89,10
Yvaa 8,112,0,16
U 124
U 125
Mvaa 8,116,4
E 124
Mvaa 8,120,8
U 126
U 127
Mvaa 8,124,12
E 125
Yvaa 12,120,24,16
E 126
Mvaa 12,124,28
Ba 104,1,8704
C 102,124
Mvaa 12,112,16
E 127
Mvaa 12,116,20
s_add_u32 m0, s41, 0
Ba 108,1,8960
C 103,126
v_smfmac_f32_16x16x64_bf16 a[168:171], a[184:187], v[66:73], v7
buffer_load_dwordx4 v4, s[56:59], s43 offen lds
Mvva 160,42,144
U 8
U 9
Mvva 160,38,148
E 8
Mvva 164,38,156
U 10
U 11
Mvva 164,42,152
buffer_load_dwordx4 v4, s[56:59], s43 offen offset:1024 lds
E 9
Mava 212,56,152
s_xor_b32 s43, s43, 0x2000
E 10
Mava 212,52,156
Ta 130,2,8192
C 34,8
Mava 208,52,148
E 11
Mava 208,56,144
Ta 128,2,10240
C 35,10
Yvaa 8,112,32,20
U 12
U 13
Mvaa 8,116,36
E 12
Mvaa 8,120,40
U 14
U 15
Mvaa 8,124,44
E 13
Yvaa 124,120,56,20
E 14
Mvaa 124,124,60
Ta 134,2,12288
C 48,12
Mvaa 124,112,48
E 15
Mvaa 124,116,52
s_add_u32 m0, s41, 32768
Ta 132,2,14336
C 49,14
v_smfmac_f32_16x16x64_bf16 a[172:175], a[184:187], v[80:87], v7
buffer_load_dwordx4 v28, s[60:63], s44 offen lds
Mvva 168,70,144
U 8
U 9
Mvva 168,66,148
E 8
Mvva 172,66,156
s_add_u32 m0, s41, 31744
U 10
U 11
Mvva 172,70,152
buffer_load_dwordx4 v28, s[60:63], s44 offen offset:2048 lds
E 9
Mava 220,84,152
s_xor_b32 s44, s44, 0x80
E 10
Mava 220,80,156
Ta 142,3,12288
C 62,8
Mava 216,80,148
E 11
Mava 216,84,144
Ta 140,3,14336
C 63,10
Yvaa 8,112,64,24
U 124
U 125
Mvaa 8,116,68
E 124
Mvaa 8,120,72
U 126
U 127
Mvaa 8,124,76
E 125
Yvaa 12,120,88,24
E 126
Mvaa 12,124,92
Ta 138,3,8192
C 76,124
Mvaa 12,112,80
E 127
Mvaa 12,116,84
Ta 136,3,10240
C 77,126
Mvva 176,98,144
U 8
U 9
Mvva 176,94,148
E 8
Mvva 180,94,156
U 10
U 11
Mvva 180,98,152
E 9
Mava 228,112,152
s_waitcnt lgkmcnt(0)
E 10
Mava 228,108,156
Ba 112,1,12288
C 90,8
Mava 224,108,148
E 11
Mava 224,112,144
Ba 116,1,12544
C 91,10
Yvaa 8,96,0,16
U 12
U 13
Mvaa 8,100,4
E 12
Mvaa 8,104,8
U 14
U 15
Mvaa 8,108,12
E 13
Yvaa 124,104,24,16
E 14
Mvaa 124,108,28
Ba 120,1,12800
C 104,12
Mvaa 124,96,16
E 15
Mvaa 124,100,20
Ba 124,1,13056
C 105,14
Mvva 184,42,128
U 8
U 9
Mvva 184,38,132
E 8
Mvva 188,38,140
U 10
U 11
Mvva 188,42,136
E 9
Mava 236,56,136
E 10
Mava 236,52,140
Ta 146,2,8704
C 36,8
Mava 232,52,132
E 11
Mava 232,56,128
Ta 144,2,10752
C 37,10
Yvaa 8,96,32,20
U 124
U 125
Mvaa 8,100,36
E 124
Mvaa 8,104,40
U 126
U 127
Mvaa 8,108,44
E 125
Yvaa 12,104,56,20
E 126
Mvaa 12,108,60
Ta 150,2,12800
C 50,124
Mvaa 12,96,48
E 127
Mvaa 12,100,52
Ta 148,2,14848
C 51,126
Mvva 192,70,128
U 8
U 9
Mvva 192,66,132
buffer_store_dwordx4 a[188:191], v5, s[68:71], s46 offen sc1 nt
E 8
U 10
Mvva 196,66,140
s_xor_b32 s46, s46, 0x1000
Tv 116,30,19456
U 11
Mvva 196,70,136
Tv 118,30,19520
E 9
Mava 244,84,136
E 10
Mava 244,80,140
Ta 158,3,12800
C 64,8
Mava 240,80,132
E 11
Mava 240,84,128
Ta 156,3,14848
C 65,10
Yvaa 8,96,64,24
U 12
U 13
Mvaa 8,100,68
E 12
Mvaa 8,104,72
U 14
U 15
Mvaa 8,108,76
E 13
Yvaa 124,104,88,24
E 14
Mvaa 124,108,92
Ta 154,3,8704
C 78,12
Mvaa 124,96,80
E 15
Mvaa 124,100,84
s_add_u32 m0, s42, 18432
Ta 152,3,10752
C 79,14
v_smfmac_f32_16x16x64_bf16 a[176:179], a[184:187], v[94:101], v7
buffer_load_dwordx4 v0, s[64:67], s45 offen lds
Mvva 200,98,128
s_xor_b32 s45, s45, 0x1000
U 8
U 9
Mvva 200,94,132
E 8
Mvva 204,94,140
U 10
U 11
Mvva 204,98,136
s_waitcnt vmcnt(13)
E 9
Mava 252,112,136
s_waitcnt lgkmcnt(0)
E 10
Mava 252,108,140
s_barrier
Ba 96,1,16384
C 92,8
Mava 248,108,132
E 11
Mava 248,112,128
Ba 100,1,16640
C 93,10
Yvaa 8,112,0,16
U 124
U 125
Mvaa 8,116,4
E 124
Mvaa 8,120,8
U 126
U 127
Mvaa 8,124,12
E 125
Yvaa 12,120,24,16
E 126
Mvaa 12,124,28
Ba 104,1,16896
C 106,124
Mvaa 12,112,16
E 127
Mvaa 12,116,20
s_add_u32 m0, s41, 8192
Ba 108,1,17152
C 107,126
v_smfmac_f32_16x16x64_bf16 a[180:183], a[184:187], v[108:115], v7
buffer_load_dwordx4 v4, s[56:59], s43 offen lds
Mvva 208,42,144
U 8
U 9
Mvva 208,38,148
E 8
Mvva 212,38,156
U 10
U 11
Mvva 212,42,152
buffer_load_dwordx4 v4, s[56:59], s43 offen offset:1024 lds
E 9
Mvva 220,56,152
s_xor_b32 s43, s43, 0x6000
E 10
Mvva 220,52,156
Ta 128,2,16384
C 38,8
Mvva 216,52,148
E 11
Mvva 216,56,144
Ta 130,2,18432
C 39,10
Yvaa 8,112,32,20
U 12
U 13
Mvaa 8,116,36
E 12
Mvaa 8,120,40
s_add_u32 m0, s41, 40960
U 14
U 15
Mvaa 8,124,44
LPAD13%=:
s_cbranch_vccnz LRET%=
buffer_load_dwordx4 v28, s[60:63], s44 offen lds
E 13
Yvaa 124,120,56,20
E 14
Mvaa 124,124,60
Ta 132,2,20480
C 52,12
Mvaa 124,112,48
E 15
Mvaa 124,116,52
Ta 134,2,22528
C 53,14
Mvva 224,70,144
)ASM"
R"ASM(
U 8
U 9
Mvva 224,66,148
E 8
Mvva 228,66,156
s_add_u32 m0, s41, 39936
U 10
U 11
Mvva 228,70,152
buffer_load_dwordx4 v28, s[60:63], s44 offen offset:2048 lds
E 9
Mvva 236,84,152
s_xor_b32 s44, s44, 0x1c080
E 10
Mvva 236,80,156
s_add_u32 s44, s44, 0x200000
Ta 140,3,20480
C 66,8
Mvva 232,80,148
s_cmp_eq_u32 s47, 3
E 11
Mvva 232,84,144
s_cselect_b32 s62, 0, s62
Ta 142,3,22528
C 67,10
Yvaa 8,112,64,24
U 124
U 125
Mvaa 8,116,68
E 124
Mvaa 8,120,72
U 126
U 127
Mvaa 8,124,76
E 125
Yvaa 12,120,88,24
E 126
Mvaa 12,124,92
Ta 136,3,16384
C 80,124
Mvaa 12,112,80
E 127
Mvaa 12,116,84
Ta 138,3,18432
C 81,126
v_add_u32 v120, LFIN%=+4096-LPF%=, v122
Mvva 240,98,144
U 8
U 9
Mvva 240,94,148
buffer_load_dword v120, v120, s[72:75], 0 offen
E 8
Mvva 244,94,156
U 10
U 11
Mvva 244,98,152
E 9
Mvva 252,112,152
s_waitcnt lgkmcnt(0)
E 10
Mvva 252,108,156
Ba 112,1,20480
C 94,8
Mvva 248,108,148
E 11
Mvva 248,112,144
Ba 116,1,20736
C 95,10
L2x84%=:
Yvaa 8,96,0,16
U 12
U 13
Mvaa 8,100,4
E 12
Mvaa 8,104,8
U 14
U 15
Mvaa 8,108,12
E 13
Yvaa 124,104,24,16
E 14
Mvaa 124,108,28
Ba 120,1,20992
C 108,12
Mvaa 124,96,16
E 15
Mvaa 124,100,20
Ba 124,1,21248
C 109,14
Mvva 128,32,128
U 8
U 9
Mvva 128,36,132
E 8
Mvva 132,36,140
U 10
U 11
Mvva 132,32,136
E 9
Mvva 140,46,136
E 10
Mvva 140,50,140
Ta 144,2,16896
C 44,8
Mvva 136,50,132
E 11
Mvva 136,46,128
Ta 146,2,18944
C 45,10
Yvaa 8,96,32,20
U 124
U 125
Mvaa 8,100,36
E 124
Mvaa 8,104,40
U 126
U 127
Mvaa 8,108,44
E 125
Yvaa 12,104,56,20
E 126
Mvaa 12,108,60
Ta 148,2,20992
C 58,124
Mvaa 12,96,48
E 127
Mvaa 12,100,52
Ta 150,2,23040
C 59,126
Mvva 144,60,128
U 8
U 9
Mvva 144,64,132
buffer_store_dwordx4 v[116:119], v5, s[68:71], s46 offen sc1 nt
E 8
U 10
Mvva 148,64,140
s_xor_b32 s46, s46, 0x7000
Ta 188,30,20480
U 11
Mvva 148,60,136
Ta 190,30,20544
E 9
Mava 196,74,136
E 10
Mava 196,78,140
Ta 156,3,20992
C 72,8
Mava 192,78,132
E 11
Mava 192,74,128
Ta 158,3,23040
C 73,10
Yvaa 8,96,64,24
U 12
U 13
Mvaa 8,100,68
E 12
Mvaa 8,104,72
U 14
U 15
Mvaa 8,108,76
E 13
Yvaa 124,104,88,24
E 14
Mvaa 124,108,92
Ta 152,3,16896
C 86,12
Mvaa 124,96,80
E 15
Mvaa 124,100,84
s_add_u32 m0, s42, 19456
Ta 154,3,18944
C 87,14
v_smfmac_f32_16x16x64_bf16 a[160:163], a[184:187], v[32:39], v7
buffer_load_dwordx4 v0, s[64:67], s45 offen lds
Mvva 152,88,128
s_xor_b32 s45, s45, 0x7000
U 8
U 9
Mvva 152,92,132
E 8
Mvva 156,92,140
U 10
U 11
Mvva 156,88,136
s_waitcnt vmcnt(13)
E 9
Mava 204,102,136
s_waitcnt lgkmcnt(0)
E 10
Mava 204,106,140
s_barrier
Ba 96,1,24576
C 100,8
Mava 200,106,132
E 11
Mava 200,102,128
Ba 100,1,24832
C 101,10
Yvaa 8,112,0,16
U 124
U 125
Mvaa 8,116,4
E 124
Mvaa 8,120,8
U 126
U 127
Mvaa 8,124,12
E 125
Yvaa 12,120,24,16
E 126
Mvaa 12,124,28
Ba 104,1,25088
C 114,124
Mvaa 12,112,16
E 127
Mvaa 12,116,20
s_add_u32 m0, s41, 16384
Ba 108,1,25344
C 115,126
v_smfmac_f32_16x16x64_bf16 a[164:167], a[184:187], v[46:53], v7
buffer_load_dwordx4 v4, s[56:59], s43 offen lds
Mvva 160,32,144
U 8
U 9
Mvva 160,36,148
E 8
Mvva 164,36,156
U 10
U 11
Mvva 164,32,152
buffer_load_dwordx4 v4, s[56:59], s43 offen offset:1024 lds
E 9
Mava 212,46,152
s_xor_b32 s43, s43, 0x2000
E 10
Mava 212,50,156
Ta 128,2,24576
C 42,8
Mava 208,50,148
E 11
Mava 208,46,144
Ta 130,2,26624
C 43,10
Yvaa 8,112,32,20
U 12
U 13
Mvaa 8,116,36
E 12
Mvaa 8,120,40
U 14
U 15
Mvaa 8,124,44
E 13
Yvaa 124,120,56,20
E 14
Mvaa 124,124,60
Ta 132,2,28672
C 56,12
Mvaa 124,112,48
E 15
Mvaa 124,116,52
s_add_u32 m0, s41, 49152
Ta 134,2,30720
C 57,14
v_smfmac_f32_16x16x64_bf16 a[168:171], a[184:187], v[60:67], v7
buffer_load_dwordx4 v28, s[60:63], s44 offen lds
Mvva 168,60,144
U 8
U 9
Mvva 168,64,148
E 8
Mvva 172,64,156
s_add_u32 m0, s41, 48128
U 10
U 11
Mvva 172,60,152
buffer_load_dwordx4 v28, s[60:63], s44 offen offset:2048 lds
E 9
Mava 220,74,152
s_xor_b32 s44, s44, 0x80
E 10
Mava 220,78,156
Ta 140,3,28672
C 70,8
Mava 216,78,148
E 11
Mava 216,74,144
Ta 142,3,30720
C 71,10
Yvaa 8,112,64,24
U 124
U 125
Mvaa 8,116,68
buffer_store_dwordx4 a[188:191], v5, s[68:71], s46 offen sc1 nt
E 124
Mvaa 8,120,72
s_xor_b32 s46, s46, 0x1000
U 126
U 127
Mvaa 8,124,76
E 125
Yvaa 12,120,88,24
E 126
Mvaa 12,124,92
Ta 136,3,24576
C 84,124
Mvaa 12,112,80
E 127
Mvaa 12,116,84
Ta 138,3,26624
C 85,126
Mvva 176,88,144
s_add_u32 m0, s42, 20480
U 8
U 9
Mvva 176,92,148
buffer_load_dwordx4 v4, s[64:67], s45 offen lds
E 8
Mvva 180,92,156
s_xor_b32 s45, s45, 0x1000
U 10
U 11
Mvva 180,88,152
E 9
Mava 228,102,152
s_waitcnt lgkmcnt(0)
E 10
Mava 228,106,156
Ba 112,1,28672
C 98,8
Mava 224,106,148
E 11
Mava 224,102,144
Ba 116,1,28928
C 99,10
s_cmp_eq_u32 s47, 3
s_cbranch_scc1 LFIN%=
L2x90%=:
Yvaa 8,96,0,16
U 12
U 13
Mvaa 8,100,4
E 12
Mvaa 8,104,8
U 14
U 15
Mvaa 8,108,12
E 13
Yvaa 124,104,24,16
E 14
Mvaa 124,108,28
Ba 120,1,29184
C 112,12
Mvaa 124,96,16
E 15
Mvaa 124,100,20
Ba 124,1,29440
C 113,14
Mvva 184,32,128
U 8
U 9
Mvva 184,36,132
E 8
Mvva 188,36,140
U 10
U 11
Mvva 188,32,136
E 9
Mava 236,46,136
E 10
Mava 236,50,140
Ta 144,2,25088
C 40,8
Mava 232,50,132
E 11
Mava 232,46,128
Ta 146,2,27136
C 41,10
Yvaa 8,96,32,20
U 124
U 125
Mvaa 8,100,36
E 124
Mvaa 8,104,40
U 126
U 127
Mvaa 8,108,44
Tv 116,30,21504
Tv 118,30,21568
E 125
Yvaa 12,104,56,20
E 126
Mvaa 12,108,60
Ta 148,2,29184
C 54,124
Mvaa 12,96,48
E 127
Mvaa 12,100,52
)ASM"
R"ASM(
Ta 150,2,31232
C 55,126
Mvva 192,60,128
s_waitcnt lgkmcnt(2)
U 8
U 9
Mvva 192,64,132
buffer_store_dwordx4 v[116:119], v5, s[68:71], s46 offen sc1 nt
E 8
Mvva 196,64,140
s_xor_b32 s46, s46, 0x3000
U 10
U 11
Mvva 196,60,136
E 9
Mava 244,74,136
E 10
Mava 244,78,140
Ta 156,3,29184
C 68,8
Mava 240,78,132
E 11
Mava 240,74,128
Ta 158,3,31232
C 69,10
Yvaa 8,96,64,24
U 12
U 13
Mvaa 8,100,68
E 12
Mvaa 8,104,72
U 14
U 15
Mvaa 8,108,76
E 13
Yvaa 124,104,88,24
E 14
Mvaa 124,108,92
Ta 152,3,25088
C 82,12
Mvaa 124,96,80
E 15
Mvaa 124,100,84
s_add_u32 m0, s42, 21504
Ta 154,3,27136
C 83,14
v_smfmac_f32_16x16x64_bf16 a[172:175], a[184:187], v[74:81], v7
buffer_load_dwordx4 v4, s[64:67], s45 offen lds
Mvva 200,88,128
s_xor_b32 s45, s45, 0x3000
U 8
U 9
Mvva 200,92,132
E 8
Mvva 204,92,140
U 10
U 11
Mvva 204,88,136
s_waitcnt vmcnt(10)
E 9
Mava 252,102,136
s_waitcnt lgkmcnt(0)
E 10
Mava 252,106,140
s_barrier
Ba 96,1,0
C 96,8
Mava 248,106,132
E 11
Mava 248,102,128
Ba 100,1,256
C 97,10
Yvaa 8,112,0,16
U 124
U 125
Mvaa 8,116,4
E 124
Mvaa 8,120,8
U 126
U 127
Mvaa 8,124,12
Ba 0,6,0
E 125
Yvaa 12,120,24,16
E 126
Mvaa 12,124,28
Ba 104,1,512
C 110,124
Mvaa 12,112,16
E 127
Mvaa 12,116,20
s_add_u32 m0, s41, 24576
Ba 108,1,768
Ba 4,6,256
C 111,126
v_smfmac_f32_16x16x64_bf16 a[176:179], a[184:187], v[88:95], v7
buffer_load_dwordx4 v4, s[56:59], s43 offen lds
Mvva 208,32,144
U 8
U 9
Mvva 208,36,148
Ba 8,6,512
E 8
Mvva 212,36,156
U 10
U 11
Mvva 212,32,152
buffer_load_dwordx4 v4, s[56:59], s43 offen offset:1024 lds
Ba 12,6,768
E 9
Mvva 220,46,152
s_xor_b32 s43, s43, 0xe000
E 10
Mvva 220,50,156
Ta 130,2,0
C 38,8
Mvva 216,50,148
E 11
Mvva 216,46,144
Ta 128,2,2048
C 39,10
Yvaa 8,112,32,20
U 12
U 13
Mvaa 8,116,36
E 12
Mvaa 8,120,40
U 14
U 15
Mvaa 8,124,44
Ba 16,6,4096
E 13
Yvaa 124,120,56,20
E 14
Mvaa 124,124,60
Ta 134,2,4096
C 52,12
Mvaa 124,112,48
E 15
Mvaa 124,116,52
s_add_u32 m0, s41, 57344
Ta 132,2,6144
Ba 20,6,4352
C 53,14
v_smfmac_f32_16x16x64_bf16 a[180:183], a[184:187], v[102:109], v7
buffer_load_dwordx4 v28, s[60:63], s44 offen lds
Mvva 224,60,144
U 8
U 9
Mvva 224,64,148
Ba 24,6,4608
E 8
Mvva 228,64,156
s_add_u32 m0, s41, 56320
U 10
U 11
Mvva 228,60,152
buffer_load_dwordx4 v28, s[60:63], s44 offen offset:2048 lds
Ba 28,6,4864
E 9
Mvva 236,74,152
s_xor_b32 s44, s44, 0x4080
E 10
Mvva 236,78,156
Ta 142,3,4096
C 66,8
Mvva 232,78,148
E 11
Mvva 232,74,144
Ta 140,3,6144
C 67,10
Yvaa 8,112,64,24
U 124
U 125
Mvaa 8,116,68
E 124
Mvaa 8,120,72
U 126
U 127
Mvaa 8,124,76
E 125
Yvaa 12,120,88,24
E 126
Mvaa 12,124,92
Ta 138,3,0
C 80,124
Mvaa 12,112,80
E 127
Mvaa 12,116,84
Ta 136,3,2048
C 81,126
Mvva 240,88,144
U 8
U 9
Mvva 240,92,148
E 8
Mvva 244,92,156
U 10
U 11
Mvva 244,88,152
E 9
Mvva 252,102,152
s_waitcnt lgkmcnt(0)
E 10
Mvva 252,106,156
Ba 112,1,4096
C 94,8
Mvva 248,106,148
E 11
Mvva 248,102,144
Ba 116,1,4352
C 95,10
s_branch LT%=
.p2align 12
.fill 0, 4, 0xbf800000
LFIN%=:
Yvaa 8,96,0,16
LPAD14%=:
s_cbranch_vccnz LRET%=
Ta 188,30,21504
Ta 190,30,21568
U 12
U 13
Mvaa 8,100,4
Ba 120,1,29184
E 12
Mvaa 8,104,8
Tv 116,30,22528
Tv 118,30,22592
U 14
U 15
Mvaa 8,108,12
Ba 124,1,29440
E 13
Yvaa 124,104,24,16
Ta 144,2,25088
E 14
Mvaa 124,108,28
Ta 146,2,27136
C 112,12
Mvaa 124,96,16
Ta 148,2,29184
E 15
Mvaa 124,100,20
Ta 150,2,31232
C 113,14
Mvva 184,32,128
Ta 156,3,29184
U 8
U 9
Mvva 184,36,132
s_waitcnt lgkmcnt(9)
buffer_store_dwordx4 a[188:191], v5, s[68:71], s46 offen sc1 nt
E 8
Mvva 188,36,140
s_xor_b32 s46, s46, 0x3000
U 10
U 11
Mvva 188,32,136
Ta 158,3,31232
E 9
Mava 236,46,136
Ta 152,3,25088
E 10
Mava 236,50,140
s_waitcnt lgkmcnt(8)
buffer_store_dwordx4 v[116:119], v5, s[68:71], s46 offen sc1 nt
C 40,8
Mava 232,50,132
s_xor_b32 s46, s46, 0x1000
E 11
Mava 232,46,128
s_waitcnt lgkmcnt(7)
Ta 154,3,27136
C 41,10
Yvaa 8,112,0,16
Ta 188,30,23552
Ta 190,30,23616
U 124
U 125
Mvaa 8,116,4
E 124
Mvaa 8,120,8
U 126
U 127
Mvaa 8,124,12
s_waitcnt vmcnt(10)
E 125
Yvaa 12,120,24,16
s_barrier
Ta 2,2,0
E 126
Mvaa 12,124,28
Ta 0,2,2048
Ta 6,2,4096
C 54,124
Mvaa 12,112,16
Ta 4,2,6144
E 127
Mvaa 12,116,20
s_waitcnt lgkmcnt(6)
Ta 14,3,4096
Ta 12,3,6144
C 55,126
Mvva 208,32,144
Ta 10,3,0
U 8
U 9
Mvva 208,36,148
s_waitcnt lgkmcnt(7)
buffer_store_dwordx4 a[188:191], v5, s[68:71], s46 offen sc1 nt
E 8
Mvva 212,36,156
s_xor_b32 s46, s46, 0x7000
s_add_u32 s46, s46, 0x2f0000
Ta 8,3,2048
Ta 18,2,512
U 10
U 11
Mvva 212,32,152
Ta 16,2,2560
E 9
Mvva 220,46,152
Ta 22,2,4608
Ta 20,2,6656
E 10
Mvva 220,50,156
C 38,8
Mvva 216,50,148
Ta 30,3,4608
Ta 28,3,6656
E 11
Mvva 216,46,144
Ta 26,3,512
Ta 24,3,2560
C 39,10
s_nop 1
v_smfmac_f32_16x16x64_bf16 a[160:163], a[184:187], v[38:45], v7
s_waitcnt lgkmcnt(8)
Mvva 128,42,0
Ta 2,2,8192
Ta 0,2,10240
U 12
U 13
Mvva 128,38,4
s_waitcnt vmcnt(3)
Ta 6,2,12288
Ta 4,2,14336
E 12
Mvva 132,38,12
Ta 14,3,12288
Ta 12,3,14336
U 14
U 15
v_accvgpr_read_b32 v124, a160
v_accvgpr_read_b32 v125, a161
v_add_f32 v124, v124, v125
v_rcp_f32 v120, v124
Mvva 132,42,8
s_waitcnt lgkmcnt(6)
ds_write_b32 v121, v120 offset:0
Bv 32,31,0
Ta 10,3,8192
Ta 8,3,10240
E 13
v_mul_f32 v125, 0x53800000, v120
v_maximum3_f32 v123, v124, v125, v124
Mvva 160,42,16
Ta 18,2,8704
Ta 16,2,10752
E 14
Mvva 160,38,20
Ta 22,2,12800
Ta 20,2,14848
C 52,12
Mvva 164,38,28
Ta 30,3,12800
Ta 28,3,14848
E 15
Mvva 164,42,24
Ta 26,3,8704
Ta 24,3,10752
C 53,14
s_waitcnt lgkmcnt(8)
v_pk_mul_f32 v[128:129], v[128:129], v[32:33] 
v_pk_mul_f32 v[132:133], v[132:133], v[32:33] 
v_pk_mul_f32 v[130:131], v[130:131], v[34:35] 
v_pk_mul_f32 v[134:135], v[134:135], v[34:35] 
v_cvt_pk_bf16_f32 v128, v128, v132
v_cvt_pk_bf16_f32 v129, v129, v133
Mvva 184,42,0
v_cvt_pk_bf16_f32 v130, v130, v134
v_cvt_pk_bf16_f32 v131, v131, v135
Mvva 184,38,4
ds_write_b128 v29, v[128:131] offset:0
v_pk_mul_f32 v[160:161], v[160:161], v[32:33] 
v_pk_mul_f32 v[164:165], v[164:165], v[32:33] 
v_pk_mul_f32 v[162:163], v[162:163], v[34:35] 
v_pk_mul_f32 v[166:167], v[166:167], v[34:35] 
v_cvt_pk_bf16_f32 v160, v160, v164
)ASM"
R"ASM(
v_cvt_pk_bf16_f32 v161, v161, v165
Mvva 188,38,12
v_cvt_pk_bf16_f32 v162, v162, v166
v_cvt_pk_bf16_f32 v163, v163, v167
Mvva 188,42,8
ds_write_b128 v29, v[160:163] offset:256
s_waitcnt lgkmcnt(2)
Mvva 208,42,16
Mvva 208,38,20
v_pk_mul_f32 v[184:185], v[184:185], v[32:33] 
s_nop 2
v_pk_mul_f32 v[188:189], v[188:189], v[32:33] 
v_pk_mul_f32 v[186:187], v[186:187], v[34:35] 
v_pk_mul_f32 v[190:191], v[190:191], v[34:35] 
v_cvt_pk_bf16_f32 v184, v184, v188
v_cvt_pk_bf16_f32 v185, v185, v189
Mvva 212,38,28
v_cvt_pk_bf16_f32 v186, v186, v190
v_cvt_pk_bf16_f32 v187, v187, v191
Mvva 212,42,24
ds_write_b128 v29, v[184:187] offset:512
v_smfmac_f32_16x16x64_bf16 a[164:167], a[184:187], v[52:59], v7
Tv 130,2,0
Tv 128,2,2048
Mava 232,56,0
Tv 134,2,4096
Tv 132,2,6144
v_pk_mul_f32 v[208:209], v[208:209], v[32:33] 
v_pk_mul_f32 v[212:213], v[212:213], v[32:33] 
v_pk_mul_f32 v[210:211], v[210:211], v[34:35] 
v_pk_mul_f32 v[214:215], v[214:215], v[34:35] 
v_cvt_pk_bf16_f32 v208, v208, v212
v_cvt_pk_bf16_f32 v209, v209, v213
Mava 232,52,4
Tv 166,3,4096
Tv 164,3,6144
v_cvt_pk_bf16_f32 v210, v210, v214
v_cvt_pk_bf16_f32 v211, v211, v215
Mava 236,52,12
ds_write_b128 v29, v[208:211] offset:768
Tv 8,30,0
Tv 10,30,64
Tv 162,3,0
Tv 160,3,2048
v_accvgpr_read_b32 v124, a164
v_accvgpr_read_b32 v125, a165
v_add_f32 v124, v124, v125
v_rcp_f32 v120, v124
Mava 236,56,8
ds_write_b32 v121, v120 offset:4096
Bv 46,31,4096
Ta 188,30,1024
Ta 190,30,1088
Tv 186,2,512
Tv 184,2,2560
v_mul_f32 v125, 0x53800000, v120
v_maximum3_f32 v123, v124, v125, v123
Mvva 216,56,16
Tv 116,30,2048
Tv 118,30,2112
Tv 190,2,4608
Tv 188,2,6656
Mvva 216,52,20
s_waitcnt lgkmcnt(12)
buffer_store_dwordx4 v[8:11], v5, s[68:71], s46 offen sc1 nt
Ta 160,30,3072
Ta 162,30,3136
Mvva 220,52,28
s_xor_b32 s46, s46, 0x1000
Mvva 220,56,24
s_waitcnt lgkmcnt(8)
buffer_store_dwordx4 a[188:191], v5, s[68:71], s46 offen sc1 nt
Tv 214,3,4608
Tv 212,3,6656
v_accvgpr_read_b32 v32, a232
v_accvgpr_read_b32 v33, a233
v_accvgpr_read_b32 v34, a234
v_accvgpr_read_b32 v35, a235
v_accvgpr_read_b32 v36, a236
v_accvgpr_read_b32 v37, a237
v_accvgpr_read_b32 v38, a238
v_accvgpr_read_b32 v39, a239
v_pk_mul_f32 v[32:33], v[32:33], v[46:47] 
v_pk_mul_f32 v[36:37], v[36:37], v[46:47] 
v_pk_mul_f32 v[34:35], v[34:35], v[48:49] 
v_pk_mul_f32 v[38:39], v[38:39], v[48:49] 
v_cvt_pk_bf16_f32 v32, v32, v36
Mvvv 136,56,128
s_xor_b32 s46, s46, 0x3000
Tv 210,3,512
Tv 208,3,2560
v_cvt_pk_bf16_f32 v33, v33, v37
v_cvt_pk_bf16_f32 v34, v34, v38
Mvvv 136,52,132
s_waitcnt lgkmcnt(8)
buffer_store_dwordx4 v[116:119], v5, s[68:71], s46 offen sc1 nt
v_cvt_pk_bf16_f32 v35, v35, v39
Mvvv 140,52,164
s_xor_b32 s46, s46, 0x1000
Mvvv 140,56,160
s_waitcnt lgkmcnt(4)
ds_write_b128 v29, v[32:35] offset:4608
buffer_store_dwordx4 a[160:163], v5, s[68:71], s46 offen sc1 nt
v_pk_mul_f32 v[216:217], v[216:217], v[46:47] 
v_pk_mul_f32 v[220:221], v[220:221], v[46:47] 
v_pk_mul_f32 v[218:219], v[218:219], v[48:49] 
v_pk_mul_f32 v[222:223], v[222:223], v[48:49] 
v_cvt_pk_bf16_f32 v216, v216, v220
s_waitcnt lgkmcnt(1)
Mavv 208,56,184
s_xor_b32 s46, s46, 0x7000
v_cvt_pk_bf16_f32 v217, v217, v221
v_cvt_pk_bf16_f32 v218, v218, v222
Mavv 208,52,188
v_cvt_pk_bf16_f32 v219, v219, v223
Mavv 212,52,212
Mavv 212,56,208
ds_write_b128 v29, v[216:219] offset:4864
v_pk_mul_f32 v[136:137], v[136:137], v[46:47] 
v_pk_mul_f32 v[140:141], v[140:141], v[46:47] 
v_pk_mul_f32 v[138:139], v[138:139], v[48:49] 
v_pk_mul_f32 v[142:143], v[142:143], v[48:49] 
v_cvt_pk_bf16_f32 v136, v136, v140
Yvaa 8,96,32,20
v_cvt_pk_bf16_f32 v137, v137, v141
v_cvt_pk_bf16_f32 v138, v138, v142
Mvaa 8,100,36
v_cvt_pk_bf16_f32 v139, v139, v143
Mvaa 8,104,40
Mvaa 8,108,44
ds_write_b128 v29, v[136:139] offset:4096
v_accvgpr_read_b32 v32, a208
v_accvgpr_read_b32 v33, a209
v_accvgpr_read_b32 v34, a210
v_accvgpr_read_b32 v35, a211
v_accvgpr_read_b32 v36, a212
v_accvgpr_read_b32 v37, a213
v_accvgpr_read_b32 v38, a214
v_accvgpr_read_b32 v39, a215
v_pk_mul_f32 v[32:33], v[32:33], v[46:47] 
v_pk_mul_f32 v[36:37], v[36:37], v[46:47] 
v_pk_mul_f32 v[34:35], v[34:35], v[48:49] 
v_pk_mul_f32 v[38:39], v[38:39], v[48:49] 
v_cvt_pk_bf16_f32 v32, v32, v36
Yvaa 124,104,56,20
v_cvt_pk_bf16_f32 v33, v33, v37
v_cvt_pk_bf16_f32 v34, v34, v38
Mvaa 124,108,60
v_cvt_pk_bf16_f32 v35, v35, v39
Mvaa 124,96,48
Mvaa 124,100,52
ds_write_b128 v29, v[32:35] offset:4352
v_smfmac_f32_16x16x64_bf16 a[172:175], a[184:187], v[74:81], v7
Ta 188,30,4096
Ta 190,30,4160
Mvva 192,60,128
Tv 116,30,5120
Tv 118,30,5184
U 8
U 9
Mvva 192,64,132
Ta 160,30,6144
Ta 162,30,6208
E 8
Mvva 196,64,140
Ta 164,30,7168
Ta 166,30,7232
U 10
U 11
Mvva 196,60,136
s_waitcnt lgkmcnt(6)
buffer_store_dwordx4 a[188:191], v5, s[68:71], s46 offen sc1 nt
E 9
Mava 244,74,136
s_xor_b32 s46, s46, 0x1000
E 10
Mava 244,78,140
s_waitcnt lgkmcnt(4)
buffer_store_dwordx4 v[116:119], v5, s[68:71], s46 offen sc1 nt
C 68,8
Mava 240,78,132
s_xor_b32 s46, s46, 0x3000
E 11
Mava 240,74,128
s_waitcnt lgkmcnt(2)
buffer_store_dwordx4 a[160:163], v5, s[68:71], s46 offen sc1 nt
C 69,10
Yvaa 8,112,32,20
s_xor_b32 s46, s46, 0x1000
U 124
U 125
Mvaa 8,116,36
s_waitcnt lgkmcnt(0)
buffer_store_dwordx4 a[164:167], v5, s[68:71], s46 offen sc1 nt
E 124
Mvaa 8,120,40
s_xor_b32 s46, s46, 0x7000
s_add_u32 s46, s46, 0x8000
U 126
U 127
Mvaa 8,124,44
E 125
Yvaa 12,120,56,20
E 126
Mvaa 12,124,60
C 82,124
Mvaa 12,112,48
E 127
Mvaa 12,116,52
C 83,126
Mvva 224,60,144
U 8
U 9
Mvva 224,64,148
E 8
Mvva 228,64,156
U 10
U 11
Mvva 228,60,152
E 9
Mvva 236,74,152
E 10
Mvva 236,78,156
C 66,8
Mvva 232,78,148
E 11
Mvva 232,74,144
C 67,10
s_nop 1
v_smfmac_f32_16x16x64_bf16 a[168:171], a[184:187], v[66:73], v7
Mvva 192,70,0
U 12
U 13
Mvva 192,66,4
E 12
Mvva 196,66,12
U 14
U 15
v_accvgpr_read_b32 v124, a168
v_accvgpr_read_b32 v125, a169
v_add_f32 v124, v124, v125
v_rcp_f32 v120, v124
Mvva 196,70,8
ds_write_b32 v121, v120 offset:8192
Bv 60,31,8192
E 13
v_mul_f32 v125, 0x53800000, v120
v_maximum3_f32 v123, v124, v125, v123
Mvva 224,70,16
E 14
Mvva 224,66,20
C 80,12
Mvva 228,66,28
E 15
Mvva 228,70,24
C 81,14
s_waitcnt lgkmcnt(0)
v_pk_mul_f32 v[192:193], v[192:193], v[60:61] 
v_pk_mul_f32 v[196:197], v[196:197], v[60:61] 
v_pk_mul_f32 v[194:195], v[194:195], v[62:63] 
v_pk_mul_f32 v[198:199], v[198:199], v[62:63] 
v_cvt_pk_bf16_f32 v192, v192, v196
Mvvv 144,70,128
v_cvt_pk_bf16_f32 v193, v193, v197
v_cvt_pk_bf16_f32 v194, v194, v198
Mvvv 144,66,132
v_cvt_pk_bf16_f32 v195, v195, v199
Mvvv 148,66,164
Mvvv 148,70,160
ds_write_b128 v29, v[192:195] offset:8704
v_pk_mul_f32 v[224:225], v[224:225], v[60:61] 
v_pk_mul_f32 v[228:229], v[228:229], v[60:61] 
v_pk_mul_f32 v[226:227], v[226:227], v[62:63] 
v_pk_mul_f32 v[230:231], v[230:231], v[62:63] 
v_cvt_pk_bf16_f32 v224, v224, v228
Mvvv 168,70,184
v_cvt_pk_bf16_f32 v225, v225, v229
v_cvt_pk_bf16_f32 v226, v226, v230
Mvvv 168,66,188
v_cvt_pk_bf16_f32 v227, v227, v231
Mvvv 172,66,212
Mvvv 172,70,208
ds_write_b128 v29, v[224:227] offset:8960
v_pk_mul_f32 v[144:145], v[144:145], v[60:61] 
v_pk_mul_f32 v[148:149], v[148:149], v[60:61] 
v_pk_mul_f32 v[146:147], v[146:147], v[62:63] 
v_pk_mul_f32 v[150:151], v[150:151], v[62:63] 
v_cvt_pk_bf16_f32 v144, v144, v148
v_smfmac_f32_16x16x64_bf16 a[172:175], a[184:187], v[80:87], v7
v_cvt_pk_bf16_f32 v145, v145, v149
v_cvt_pk_bf16_f32 v146, v146, v150
Mava 240,84,0
v_cvt_pk_bf16_f32 v147, v147, v151
Mava 240,80,4
Mava 244,80,12
ds_write_b128 v29, v[144:147] offset:8192
v_pk_mul_f32 v[168:169], v[168:169], v[60:61] 
v_pk_mul_f32 v[172:173], v[172:173], v[60:61] 
v_pk_mul_f32 v[170:171], v[170:171], v[62:63] 
v_pk_mul_f32 v[174:175], v[174:175], v[62:63] 
v_cvt_pk_bf16_f32 v168, v168, v172
v_accvgpr_read_b32 v124, a172
v_accvgpr_read_b32 v125, a173
v_add_f32 v124, v124, v125
v_rcp_f32 v120, v124
Mava 244,84,8
ds_write_b32 v121, v120 offset:12288
Bv 74,31,12288
v_cvt_pk_bf16_f32 v169, v169, v173
v_cvt_pk_bf16_f32 v170, v170, v174
v_mul_f32 v125, 0x53800000, v120
v_maximum3_f32 v123, v124, v125, v123
Mvva 232,84,16
v_cvt_pk_bf16_f32 v171, v171, v175
Mvva 232,80,20
Mvva 236,80,28
ds_write_b128 v29, v[168:171] offset:8448
Mvva 236,84,24
Tv 136,30,8192
Tv 138,30,8256
v_accvgpr_read_b32 v60, a240
v_accvgpr_read_b32 v61, a241
v_accvgpr_read_b32 v62, a242
v_accvgpr_read_b32 v63, a243
v_accvgpr_read_b32 v64, a244
v_accvgpr_read_b32 v65, a245
v_accvgpr_read_b32 v66, a246
v_accvgpr_read_b32 v67, a247
s_waitcnt lgkmcnt(3)
v_pk_mul_f32 v[60:61], v[60:61], v[74:75] 
v_pk_mul_f32 v[64:65], v[64:65], v[74:75] 
v_pk_mul_f32 v[62:63], v[62:63], v[76:77] 
v_pk_mul_f32 v[66:67], v[66:67], v[76:77] 
v_cvt_pk_bf16_f32 v60, v60, v64
Mavv 192,84,128
Tv 140,30,9216
Tv 142,30,9280
v_cvt_pk_bf16_f32 v61, v61, v65
v_cvt_pk_bf16_f32 v62, v62, v66
Mavv 192,80,132
Ta 208,30,10240
Ta 210,30,10304
v_cvt_pk_bf16_f32 v63, v63, v67
Mavv 196,80,164
Ta 212,30,11264
Ta 214,30,11328
Mavv 196,84,160
s_waitcnt lgkmcnt(6)
ds_write_b128 v29, v[60:63] offset:12800
buffer_store_dwordx4 v[136:139], v5, s[68:71], s46 offen sc1 nt
v_pk_mul_f32 v[232:233], v[232:233], v[74:75] 
v_pk_mul_f32 v[236:237], v[236:237], v[74:75] 
v_pk_mul_f32 v[234:235], v[234:235], v[76:77] 
v_pk_mul_f32 v[238:239], v[238:239], v[76:77] 
v_cvt_pk_bf16_f32 v232, v232, v236
Mavv 216,84,184
s_xor_b32 s46, s46, 0x1000
v_cvt_pk_bf16_f32 v233, v233, v237
v_cvt_pk_bf16_f32 v234, v234, v238
Mavv 216,80,188
s_waitcnt lgkmcnt(5)
buffer_store_dwordx4 v[140:143], v5, s[68:71], s46 offen sc1 nt
v_cvt_pk_bf16_f32 v235, v235, v239
Mavv 220,80,212
s_xor_b32 s46, s46, 0x3000
Mavv 220,84,208
s_waitcnt lgkmcnt(3)
ds_write_b128 v29, v[232:235] offset:13056
buffer_store_dwordx4 a[208:211], v5, s[68:71], s46 offen sc1 nt
v_accvgpr_read_b32 v32, a192
v_accvgpr_read_b32 v33, a193
v_accvgpr_read_b32 v34, a194
v_accvgpr_read_b32 v35, a195
v_accvgpr_read_b32 v36, a196
v_accvgpr_read_b32 v37, a197
v_accvgpr_read_b32 v38, a198
v_accvgpr_read_b32 v39, a199
v_pk_mul_f32 v[32:33], v[32:33], v[74:75] 
v_pk_mul_f32 v[36:37], v[36:37], v[74:75] 
v_pk_mul_f32 v[34:35], v[34:35], v[76:77] 
v_pk_mul_f32 v[38:39], v[38:39], v[76:77] 
v_cvt_pk_bf16_f32 v32, v32, v36
Yvaa 8,96,64,24
s_xor_b32 s46, s46, 0x1000
v_cvt_pk_bf16_f32 v33, v33, v37
v_cvt_pk_bf16_f32 v34, v34, v38
Mvaa 8,100,68
s_waitcnt lgkmcnt(2)
buffer_store_dwordx4 a[212:215], v5, s[68:71], s46 offen sc1 nt
v_cvt_pk_bf16_f32 v35, v35, v39
Mvaa 8,104,72
LPAD15%=:
s_cbranch_vccnz LRET%=
s_xor_b32 s46, s46, 0x7000
Mvaa 8,108,76
ds_write_b128 v29, v[32:35] offset:12288
v_accvgpr_read_b32 v60, a216
v_accvgpr_read_b32 v61, a217
v_accvgpr_read_b32 v62, a218
v_accvgpr_read_b32 v63, a219
)ASM"
R"ASM(
v_accvgpr_read_b32 v64, a220
v_accvgpr_read_b32 v65, a221
v_accvgpr_read_b32 v66, a222
v_accvgpr_read_b32 v67, a223
v_pk_mul_f32 v[60:61], v[60:61], v[74:75] 
v_pk_mul_f32 v[64:65], v[64:65], v[74:75] 
v_pk_mul_f32 v[62:63], v[62:63], v[76:77] 
v_pk_mul_f32 v[66:67], v[66:67], v[76:77] 
v_cvt_pk_bf16_f32 v60, v60, v64
Yvaa 124,104,88,24
v_cvt_pk_bf16_f32 v61, v61, v65
v_cvt_pk_bf16_f32 v62, v62, v66
Mvaa 124,108,92
v_cvt_pk_bf16_f32 v63, v63, v67
Mvaa 124,96,80
Mvaa 124,100,84
ds_write_b128 v29, v[60:63] offset:12544
v_smfmac_f32_16x16x64_bf16 a[176:179], a[184:187], v[88:95], v7
Ta 232,30,12288
Ta 234,30,12352
v_smfmac_f32_16x16x64_bf16 a[180:183], a[184:187], v[102:109], v7
Ta 236,30,13312
Ta 238,30,13376
Mvva 200,88,128
Tv 216,30,14336
Tv 218,30,14400
U 8
U 9
Mvva 200,92,132
Tv 220,30,15360
Tv 222,30,15424
E 8
Mvva 204,92,140
s_waitcnt lgkmcnt(6)
buffer_store_dwordx4 a[232:235], v5, s[68:71], s46 offen sc1 nt
U 10
U 11
Mvva 204,88,136
s_xor_b32 s46, s46, 0x1000
E 9
Mava 252,102,136
s_waitcnt lgkmcnt(4)
buffer_store_dwordx4 a[236:239], v5, s[68:71], s46 offen sc1 nt
E 10
Mava 252,106,140
s_xor_b32 s46, s46, 0x3000
C 96,8
Mava 248,106,132
s_waitcnt lgkmcnt(2)
buffer_store_dwordx4 v[216:219], v5, s[68:71], s46 offen sc1 nt
E 11
Mava 248,102,128
s_xor_b32 s46, s46, 0x1000
C 97,10
Yvaa 8,112,64,24
s_waitcnt lgkmcnt(0)
buffer_store_dwordx4 v[220:223], v5, s[68:71], s46 offen sc1 nt
U 124
U 125
Mvaa 8,116,68
s_xor_b32 s46, s46, 0x7000
s_add_u32 s46, s46, 0x8000
E 124
Mvaa 8,120,72
U 126
U 127
Mvaa 8,124,76
E 125
Yvaa 12,120,88,24
E 126
Mvaa 12,124,92
C 110,124
Mvaa 12,112,80
E 127
Mvaa 12,116,84
C 111,126
Mvva 240,88,144
U 8
U 9
Mvva 240,92,148
E 8
Mvva 244,92,156
U 10
U 11
Mvva 244,88,152
E 9
Mvva 252,102,152
E 10
Mvva 252,106,156
C 94,8
Mvva 248,106,148
E 11
Mvva 248,102,144
C 95,10
s_nop 1
v_smfmac_f32_16x16x64_bf16 a[176:179], a[184:187], v[94:101], v7
Mvva 200,98,0
U 12
U 13
Mvva 200,94,4
E 12
Mvva 204,94,12
U 14
U 15
v_accvgpr_read_b32 v124, a176
v_accvgpr_read_b32 v125, a177
v_add_f32 v124, v124, v125
v_rcp_f32 v120, v124
Mvva 204,98,8
ds_write_b32 v121, v120 offset:16384
Bv 88,31,16384
E 13
v_mul_f32 v125, 0x53800000, v120
v_maximum3_f32 v123, v124, v125, v123
Mvva 240,98,16
E 14
Mvva 240,94,20
C 108,12
Mvva 244,94,28
E 15
Mvva 244,98,24
C 109,14
s_waitcnt lgkmcnt(0)
v_pk_mul_f32 v[200:201], v[200:201], v[88:89] 
v_pk_mul_f32 v[204:205], v[204:205], v[88:89] 
v_pk_mul_f32 v[202:203], v[202:203], v[90:91] 
v_pk_mul_f32 v[206:207], v[206:207], v[90:91] 
v_cvt_pk_bf16_f32 v200, v200, v204
Mvvv 152,98,128
v_cvt_pk_bf16_f32 v201, v201, v205
v_cvt_pk_bf16_f32 v202, v202, v206
Mvvv 152,94,132
v_cvt_pk_bf16_f32 v203, v203, v207
Mvvv 156,94,164
Mvvv 156,98,160
ds_write_b128 v29, v[200:203] offset:16896
v_pk_mul_f32 v[240:241], v[240:241], v[88:89] 
v_pk_mul_f32 v[244:245], v[244:245], v[88:89] 
v_pk_mul_f32 v[242:243], v[242:243], v[90:91] 
v_pk_mul_f32 v[246:247], v[246:247], v[90:91] 
v_cvt_pk_bf16_f32 v240, v240, v244
Mvvv 176,98,184
v_cvt_pk_bf16_f32 v241, v241, v245
v_cvt_pk_bf16_f32 v242, v242, v246
Mvvv 176,94,188
v_cvt_pk_bf16_f32 v243, v243, v247
Mvvv 180,94,212
Mvvv 180,98,208
ds_write_b128 v29, v[240:243] offset:17152
v_pk_mul_f32 v[152:153], v[152:153], v[88:89] 
v_pk_mul_f32 v[156:157], v[156:157], v[88:89] 
v_pk_mul_f32 v[154:155], v[154:155], v[90:91] 
v_pk_mul_f32 v[158:159], v[158:159], v[90:91] 
v_cvt_pk_bf16_f32 v152, v152, v156
v_smfmac_f32_16x16x64_bf16 a[180:183], a[184:187], v[108:115], v7
v_cvt_pk_bf16_f32 v153, v153, v157
v_cvt_pk_bf16_f32 v154, v154, v158
Mava 248,112,0
v_cvt_pk_bf16_f32 v155, v155, v159
Mava 248,108,4
Mava 252,108,12
ds_write_b128 v29, v[152:155] offset:16384
v_pk_mul_f32 v[176:177], v[176:177], v[88:89] 
v_pk_mul_f32 v[180:181], v[180:181], v[88:89] 
v_pk_mul_f32 v[178:179], v[178:179], v[90:91] 
v_pk_mul_f32 v[182:183], v[182:183], v[90:91] 
v_cvt_pk_bf16_f32 v176, v176, v180
v_accvgpr_read_b32 v124, a180
v_accvgpr_read_b32 v125, a181
v_add_f32 v124, v124, v125
v_rcp_f32 v120, v124
Mava 252,112,8
ds_write_b32 v121, v120 offset:20480
Bv 102,31,20480
v_cvt_pk_bf16_f32 v177, v177, v181
v_cvt_pk_bf16_f32 v178, v178, v182
v_mul_f32 v125, 0x53800000, v120
v_maximum3_f32 v123, v124, v125, v123
v_cmp_nle_f32 s[48:49], v123, s51
Mvva 248,112,16
v_cvt_pk_bf16_f32 v179, v179, v183
Mvva 248,108,20
Mvva 252,108,28
ds_write_b128 v29, v[176:179] offset:16640
Mvva 252,112,24
Tv 136,30,16384
Tv 138,30,16448
v_accvgpr_read_b32 v88, a248
v_accvgpr_read_b32 v89, a249
v_accvgpr_read_b32 v90, a250
v_accvgpr_read_b32 v91, a251
v_accvgpr_read_b32 v92, a252
v_accvgpr_read_b32 v93, a253
v_accvgpr_read_b32 v94, a254
v_accvgpr_read_b32 v95, a255
s_waitcnt lgkmcnt(3)
v_pk_mul_f32 v[88:89], v[88:89], v[102:103] 
v_pk_mul_f32 v[92:93], v[92:93], v[102:103] 
v_pk_mul_f32 v[90:91], v[90:91], v[104:105] 
v_pk_mul_f32 v[94:95], v[94:95], v[104:105] 
v_cvt_pk_bf16_f32 v88, v88, v92
Mavv 200,112,128
Tv 140,30,17408
Tv 142,30,17472
v_cvt_pk_bf16_f32 v89, v89, v93
v_cvt_pk_bf16_f32 v90, v90, v94
Mavv 200,108,132
Ta 208,30,18432
Ta 210,30,18496
v_cvt_pk_bf16_f32 v91, v91, v95
Mavv 204,108,164
Ta 212,30,19456
Ta 214,30,19520
Mavv 204,112,160
s_waitcnt lgkmcnt(6)
ds_write_b128 v29, v[88:91] offset:20992
buffer_store_dwordx4 v[136:139], v5, s[68:71], s46 offen sc1 nt
v_pk_mul_f32 v[248:249], v[248:249], v[102:103] 
v_pk_mul_f32 v[252:253], v[252:253], v[102:103] 
v_pk_mul_f32 v[250:251], v[250:251], v[104:105] 
v_pk_mul_f32 v[254:255], v[254:255], v[104:105] 
v_cvt_pk_bf16_f32 v248, v248, v252
Mavv 224,112,184
s_xor_b32 s46, s46, 0x1000
v_cvt_pk_bf16_f32 v249, v249, v253
v_cvt_pk_bf16_f32 v250, v250, v254
Mavv 224,108,188
s_waitcnt lgkmcnt(5)
buffer_store_dwordx4 v[140:143], v5, s[68:71], s46 offen sc1 nt
v_cvt_pk_bf16_f32 v251, v251, v255
Mavv 228,108,212
s_xor_b32 s46, s46, 0x3000
Mavv 228,112,208
s_waitcnt lgkmcnt(3)
ds_write_b128 v29, v[248:251] offset:21248
buffer_store_dwordx4 a[208:211], v5, s[68:71], s46 offen sc1 nt
v_accvgpr_read_b32 v60, a200
v_accvgpr_read_b32 v61, a201
v_accvgpr_read_b32 v62, a202
v_accvgpr_read_b32 v63, a203
v_accvgpr_read_b32 v64, a204
v_accvgpr_read_b32 v65, a205
v_accvgpr_read_b32 v66, a206
v_accvgpr_read_b32 v67, a207
v_pk_mul_f32 v[60:61], v[60:61], v[102:103] 
v_pk_mul_f32 v[64:65], v[64:65], v[102:103] 
v_pk_mul_f32 v[62:63], v[62:63], v[104:105] 
v_pk_mul_f32 v[66:67], v[66:67], v[104:105] 
v_cvt_pk_bf16_f32 v60, v60, v64
s_xor_b32 s46, s46, 0x1000
v_cvt_pk_bf16_f32 v61, v61, v65
v_cvt_pk_bf16_f32 v62, v62, v66
s_waitcnt lgkmcnt(2)
buffer_store_dwordx4 a[212:215], v5, s[68:71], s46 offen sc1 nt
v_cvt_pk_bf16_f32 v63, v63, v67
s_xor_b32 s46, s46, 0x7000
ds_write_b128 v29, v[60:63] offset:20480
v_accvgpr_read_b32 v88, a224
v_accvgpr_read_b32 v89, a225
v_accvgpr_read_b32 v90, a226
v_accvgpr_read_b32 v91, a227
v_accvgpr_read_b32 v92, a228
v_accvgpr_read_b32 v93, a229
v_accvgpr_read_b32 v94, a230
v_accvgpr_read_b32 v95, a231
v_pk_mul_f32 v[88:89], v[88:89], v[102:103] 
v_pk_mul_f32 v[92:93], v[92:93], v[102:103] 
v_pk_mul_f32 v[90:91], v[90:91], v[104:105] 
v_pk_mul_f32 v[94:95], v[94:95], v[104:105] 
v_cvt_pk_bf16_f32 v88, v88, v92
v_cvt_pk_bf16_f32 v89, v89, v93
v_cvt_pk_bf16_f32 v90, v90, v94
v_cvt_pk_bf16_f32 v91, v91, v95
ds_write_b128 v29, v[88:91] offset:20736
Ta 232,30,20480
Ta 234,30,20544
Ta 236,30,21504
Ta 238,30,21568
Tv 216,30,22528
Tv 218,30,22592
Tv 220,30,23552
Tv 222,30,23616
s_waitcnt lgkmcnt(6)
buffer_store_dwordx4 a[232:235], v5, s[68:71], s46 offen sc1 nt
s_xor_b32 s46, s46, 0x1000
s_waitcnt lgkmcnt(4)
buffer_store_dwordx4 a[236:239], v5, s[68:71], s46 offen sc1 nt
s_xor_b32 s46, s46, 0x3000
s_waitcnt lgkmcnt(2)
buffer_store_dwordx4 v[216:219], v5, s[68:71], s46 offen sc1 nt
s_xor_b32 s46, s46, 0x1000
s_waitcnt lgkmcnt(0)
buffer_store_dwordx4 v[220:223], v5, s[68:71], s46 offen sc1 nt
s_xor_b32 s46, s46, 0x7000
s_add_u32 s46, s46, 0x2f0000
s_lshl_b64 s[54:55], s[54:55], 16
s_and_b32 s53, s48, 0xffff
s_or_b32 s54, s54, s53
s_waitcnt vmcnt(0) lgkmcnt(0)
s_cmp_lg_u64 s[54:55], 0
s_cbranch_scc1 LBAD%=
s_endpgm
LBAD%=:
s_waitcnt vmcnt(0)
s_mov_b64 %[mask], s[54:55]
LEND%=:
)ASM"
    : [mask] "=s"(mask)
    : [KA] "s"(__builtin_amdgcn_kernarg_segment_ptr()), [bid] "s"(bid), [wid] "s"(wid), [lds] "s"(ldsb)
    : "memory", "scc", "vcc", "v0", "v1", "v2", "v3", "v4", "v5", "v6", "v7", "v8", "v9", "v10", "v11", "v12", "v13", "v14", "v15", "v16", "v17", "v18", "v19", "v20", "v21", "v22", "v23", "v24", "v25", "v26", "v27", "v28", "v29", "v30", "v31", "v32", "v33", "v34", "v35", "v36", "v37", "v38", "v39", "v40", "v41", "v42", "v43", "v44", "v45", "v46", "v47", "v48", "v49", "v50", "v51", "v52", "v53", "v54", "v55", "v56", "v57", "v58", "v59", "v60", "v61", "v62", "v63", "v64", "v65", "v66", "v67", "v68", "v69", "v70", "v71", "v72", "v73", "v74", "v75", "v76", "v77", "v78", "v79", "v80", "v81", "v82", "v83", "v84", "v85", "v86", "v87", "v88", "v89", "v90", "v91", "v92", "v93", "v94", "v95", "v96", "v97", "v98", "v99", "v100", "v101", "v102", "v103", "v104", "v105", "v106", "v107", "v108", "v109", "v110", "v111", "v112", "v113", "v114", "v115", "v116", "v117", "v118", "v119", "v120", "v121", "v122", "v123", "v124", "v125", "v126", "v127", "v128", "v129", "v130", "v131", "v132", "v133", "v134", "v135", "v136", "v137", "v138", "v139", "v140", "v141", "v142", "v143", "v144", "v145", "v146", "v147", "v148", "v149", "v150", "v151", "v152", "v153", "v154", "v155", "v156", "v157", "v158", "v159", "v160", "v161", "v162", "v163", "v164", "v165", "v166", "v167", "v168", "v169", "v170", "v171", "v172", "v173", "v174", "v175", "v176", "v177", "v178", "v179", "v180", "v181", "v182", "v183", "v184", "v185", "v186", "v187", "v188", "v189", "v190", "v191", "v192", "v193", "v194", "v195", "v196", "v197", "v198", "v199", "v200", "v201", "v202", "v203", "v204", "v205", "v206", "v207", "v208", "v209", "v210", "v211", "v212", "v213", "v214", "v215", "v216", "v217", "v218", "v219", "v220", "v221", "v222", "v223", "v224", "v225", "v226", "v227", "v228", "v229", "v230", "v231", "v232", "v233", "v234", "v235", "v236", "v237", "v238", "v239", "v240", "v241", "v242", "v243", "v244", "v245", "v246", "v247", "v248", "v249", "v250", "v251", "v252", "v253", "v254", "v255", "a0", "a1", "a2", "a3", "a4", "a5", "a6", "a7", "a8", "a9", "a10", "a11", "a12", "a13", "a14", "a15", "a16", "a17", "a18", "a19", "a20", "a21", "a22", "a23", "a24", "a25", "a26", "a27", "a28", "a29", "a30", "a31", "a32", "a33", "a34", "a35", "a36", "a37", "a38", "a39", "a40", "a41", "a42", "a43", "a44", "a45", "a46", "a47", "a48", "a49", "a50", "a51", "a52", "a53", "a54", "a55", "a56", "a57", "a58", "a59", "a60", "a61", "a62", "a63", "a64", "a65", "a66", "a67", "a68", "a69", "a70", "a71", "a72", "a73", "a74", "a75", "a76", "a77", "a78", "a79", "a80", "a81", "a82", "a83", "a84", "a85", "a86", "a87", "a88", "a89", "a90", "a91", "a92", "a93", "a94", "a95", "a96", "a97", "a98", "a99", "a100", "a101", "a102", "a103", "a104", "a105", "a106", "a107", "a108", "a109", "a110", "a111", "a112", "a113", "a114", "a115", "a116", "a117", "a118", "a119", "a120", "a121", "a122", "a123", "a124", "a125", "a126", "a127", "a128", "a129", "a130", "a131", "a132", "a133", "a134", "a135", "a136", "a137", "a138", "a139", "a140", "a141", "a142", "a143", "a144", "a145", "a146", "a147", "a148", "a149", "a150", "a151", "a152", "a153", "a154", "a155", "a156", "a157", "a158", "a159", "a160", "a161", "a162", "a163", "a164", "a165", "a166", "a167", "a168", "a169", "a170", "a171", "a172", "a173", "a174", "a175", "a176", "a177", "a178", "a179", "a180", "a181", "a182", "a183", "a184", "a185", "a186", "a187", "a188", "a189", "a190", "a191", "a192", "a193", "a194", "a195", "a196", "a197", "a198", "a199", "a200", "a201", "a202", "a203", "a204", "a205", "a206", "a207", "a208", "a209", "a210", "a211", "a212", "a213", "a214", "a215", "a216", "a217", "a218", "a219", "a220", "a221", "a222", "a223", "a224", "a225", "a226", "a227", "a228", "a229", "a230", "a231", "a232", "a233", "a234", "a235", "a236", "a237", "a238", "a239", "a240", "a241", "a242", "a243", "a244", "a245", "a246", "a247", "a248", "a249", "a250", "a251", "a252", "a253", "a254", "a255", "s40", "s41", "s42", "s43", "s44", "s45", "s46", "s47", "s48", "s49", "s50", "s51", "s52", "s53", "s54", "s55", "s56", "s57", "s58", "s59", "s60", "s61", "s62", "s63", "s64", "s65", "s66", "s67", "s68", "s69", "s70", "s71", "s72", "s73", "s74", "s75", "s76", "s77", "s78", "s79", "s80", "s81", "s82", "s83");
  if (mask) {
    const unsigned lane = __builtin_amdgcn_mbcnt_hi(~0u, __builtin_amdgcn_mbcnt_lo(~0u, 0u));
    for (int r = 0; r < 4; ++r) {
      const unsigned bits = (unsigned)(mask >> (16 * (3 - r))) & 0xffffu;
      if (bits) {
        const unsigned head = (bid & 7) * 128 + r * 32 + (bid >> 3);
        const size_t qo = (size_t)head * 98304 + wid * 1024, ko = (size_t)(head >> 1) * 131072;
        exact_rows(Q + qo, K + ko, V + ko, O + qo, lane, bits, ((bid >> 3) ^ bid) & 7);
      }
    }
  }
}

extern "C" void kernel_launch(void* const* d_in, const int* in_sizes, int n_in, void* d_out, int out_size, void* d_ws, size_t ws_size, hipStream_t stream) {
  hipLaunchKernelGGL(gqa_attn_kernel, dim3(256), dim3(256), 0, stream, (const char*)d_in[0], (const char*)d_in[1], (const char*)d_in[2], (char*)d_out);
}
```
